```python
import torch

def _softmax(x, axis=-1):
    return torch.softmax(x, dim=axis)
def _gelu(x):
    return torch.nn.functional.gelu(x, approximate="tanh")
def _silu(x):
    return torch.nn.functional.silu(x)
def _sigmoid(x):
    return torch.sigmoid(x)
def _layernorm(x, axis=-1, eps=1e-5):
    m = x.mean(dim=axis, keepdim=True)
    v = x.var(dim=axis, keepdim=True, unbiased=False)
    return (x - m) / torch.sqrt(v + eps)
def _scatter_add(tgt, idx, src, axis=0):
    return tgt.clone().index_add_(axis, idx, src)
def _segment_sum(src, seg, n):
    out = torch.zeros((n,) + src.shape[1:], dtype=src.dtype)
    return out.index_add_(0, seg, src)
def _pad(x, pw):
    flat = tuple(v for pair in reversed(pw) for v in pair)
    return torch.nn.functional.pad(x, flat)

kernel_name = 'recurrent_scan__ssm_conv_s5__mixer_token_mlp__prompt'

def setup_inputs():

    _w = (lambda a: (lambda t: t.to(torch.bfloat16) if t.is_floating_point() else t)(torch.as_tensor(a)))
    def _A_setup():
        B, S, D = 1, 2048, 512
        P = D
        Lambda_re = -0.5 * (lambda _x: torch.exp(torch.as_tensor(_x)))((lambda *s: torch.randn(*s, dtype=torch.bfloat16) * 0.4)(P))
        Lambda_im = (lambda *s: torch.randn(*s, dtype=torch.bfloat16) * 0.4)(P) * 1.0
        B_re = (lambda *s: torch.randn(*s, dtype=torch.bfloat16) * 0.4)(P, D) * (D ** -0.5)
        B_im = (lambda *s: torch.randn(*s, dtype=torch.bfloat16) * 0.4)(P, D) * (D ** -0.5)
        C_re = (lambda *s: torch.randn(*s, dtype=torch.bfloat16) * 0.4)(D, P) * (P ** -0.5)
        C_im = (lambda *s: torch.randn(*s, dtype=torch.bfloat16) * 0.4)(D, P) * (P ** -0.5)
        log_dt = (lambda *s: torch.randn(*s, dtype=torch.bfloat16) * 0.4)(P) * 0.3 - 2.0
        Dskip = (lambda *s: torch.randn(*s, dtype=torch.bfloat16) * 0.2)(D)
        return {
            "x": _w((lambda *s: torch.randn(*s, dtype=torch.bfloat16) * 0.05)(B, S, D)),
            "Lambda_re": _w(Lambda_re),
            "Lambda_im": _w(Lambda_im),
            "B_re": _w(B_re),
            "B_im": _w(B_im),
            "C_re": _w(C_re),
            "C_im": _w(C_im),
            "log_dt": _w(log_dt),
            "Dskip": _w(Dskip),
        }
    _A_in = _A_setup()

    def _B_setup():
        B, S, D, F = 1, 2048, 512, 1792
        return {
            "x": _w((lambda *s: torch.randn(*s, dtype=torch.bfloat16) * 0.05)(B, S, D)),
            "w1": _w((lambda *s: torch.randn(*s, dtype=torch.bfloat16) * 0.4)(S, F) * (S ** -0.5)),
            "b1": _w((lambda *s: torch.randn(*s, dtype=torch.bfloat16) * 3.2)(F) * 0.02),
            "w2": _w((lambda *s: torch.randn(*s, dtype=torch.bfloat16) * 0.4)(F, S) * (F ** -0.5)),
            "b2": _w((lambda *s: torch.randn(*s, dtype=torch.bfloat16) * 0.8)(S) * 0.02),
        }
    _B_in = _B_setup()

    return {
        "A_x": _A_in["x"],
        "A_Lambda_re": _A_in["Lambda_re"],
        "A_Lambda_im": _A_in["Lambda_im"],
        "A_B_re": _A_in["B_re"],
        "A_B_im": _A_in["B_im"],
        "A_C_re": _A_in["C_re"],
        "A_C_im": _A_in["C_im"],
        "A_log_dt": _A_in["log_dt"],
        "A_Dskip": _A_in["Dskip"],
        "B_x": _B_in["x"],
        "B_w1": _B_in["w1"],
        "B_b1": _B_in["b1"],
        "B_w2": _B_in["w2"],
        "B_b2": _B_in["b2"],
    }

def reference(A_x, A_Lambda_re, A_Lambda_im, A_B_re, A_B_im, A_C_re, A_C_im, A_log_dt, A_Dskip, B_x, B_w1, B_b1, B_w2, B_b2):
    def _A_ref(x, Lambda_re, Lambda_im, B_re, B_im, C_re, C_im, log_dt, Dskip):
        Bsz, L, H = x.shape
        Lam = Lambda_re + 1j * Lambda_im
        Bmat = B_re + 1j * B_im
        Cmat = C_re + 1j * C_im
        dt = torch.exp(log_dt)
        Lam_bar = torch.exp(dt * Lam)
        B_bar = ((Lam_bar - 1.0) / Lam)[:, None] * Bmat
        Bu = torch.einsum("bld,pd->blp", x + 0j, B_bar)
        h = torch.zeros_like(Bu[:, 0, :])
        hs = []
        for t in range(L):
            h = Lam_bar[None, :] * h + Bu[:, t, :]
            hs.append(h)
        hseq = torch.stack(hs, 1)
        y = 2.0 * torch.einsum("blp,dp->bld", hseq, Cmat).real + x * Dskip[None, None, :]
        return y

    def _B_ref(x, w1, b1, w2, b2):
        B, S, D = x.shape
        h = _layernorm(x, axis=-1)
        u = torch.einsum("bsd,sf->bdf", h, w1) + b1
        u = _gelu(u)
        y = torch.einsum("bdf,fs->bds", u, w2) + b2
        return (lambda x, *d: x.permute(*d))(y, 0, 2, 1)

    x = A_x
    xn = _layernorm(x, axis=-1)
    s = _A_ref(xn, A_Lambda_re, A_Lambda_im, A_B_re, A_B_im, A_C_re, A_C_im, A_log_dt, A_Dskip)
    g = _silu(_B_ref(xn, B_w1, B_b1, B_w2, B_b2))
    return x + s * g
```

```cpp
#include <hip/hip_runtime.h>
#include <hip/hip_bf16.h>

#define SEQ 2048
#define DM 512
#define PP 512
#define FF 1792

typedef unsigned short u16;
typedef __attribute__((ext_vector_type(8))) short bf16x8;
typedef __attribute__((ext_vector_type(4))) short bf16x4;
typedef __attribute__((ext_vector_type(16))) float f32x16;
typedef __attribute__((ext_vector_type(4))) float f32x4;
typedef __attribute__((address_space(3))) const char* lds_cptr;
typedef __attribute__((address_space(3))) bf16x4 lds_bf16x4;
typedef __attribute__((address_space(3))) const bf16x8 lds_bf16x8;

__device__ __forceinline__ float bf2f(u16 v) { return __uint_as_float(((unsigned)v) << 16); }
__device__ __forceinline__ u16 f2bf(float f) {
  unsigned u = __float_as_uint(f);
  u += 0x7fffu + ((u >> 16) & 1u);
  return (u16)(u >> 16);
}
__device__ __forceinline__ float rbf(float f) { return bf2f(f2bf(f)); }

__device__ __forceinline__ float wave_sum(float v) {
#pragma unroll
  for (int o = 32; o > 0; o >>= 1) v += __shfl_xor(v, o);
  return v;
}

__device__ __forceinline__ void ln_bf16(const float (&in)[8], float (&out)[8]) {
  float s = 0.f;
#pragma unroll
  for (int j = 0; j < 8; ++j) s += in[j];
  s = wave_sum(s);
  float mean = s * (1.0f / DM);
  float q = 0.f;
#pragma unroll
  for (int j = 0; j < 8; ++j) { float d = in[j] - mean; q += d * d; }
  q = wave_sum(q);
  float var = rbf(q * (1.0f / DM));
  float m = rbf(mean);
  float sd = rbf(sqrtf(rbf(var + 1e-5f)));
#pragma unroll
  for (int j = 0; j < 8; ++j) out[j] = rbf(rbf(in[j] - m) / sd);
}

__global__ __launch_bounds__(256) void k_ln(const u16* __restrict__ x, u16* __restrict__ xn, u16* __restrict__ h) {
  int row = blockIdx.x * 4 + (threadIdx.x >> 6);
  int lane = threadIdx.x & 63;
  const u16* xr = x + (long)row * DM + lane * 8;
  bf16x8 v = *(const bf16x8*)xr;
  float in[8], o1[8], o2[8];
#pragma unroll
  for (int j = 0; j < 8; ++j) in[j] = bf2f((u16)v[j]);
  ln_bf16(in, o1);
  ln_bf16(o1, o2);
  bf16x8 w1, w2;
#pragma unroll
  for (int j = 0; j < 8; ++j) { w1[j] = (short)f2bf(o1[j]); w2[j] = (short)f2bf(o2[j]); }
  *(bf16x8*)(xn + (long)row * DM + lane * 8) = w1;
  *(bf16x8*)(h + (long)row * DM + lane * 8) = w2;
}

__device__ __forceinline__ void glds16(const void* g, void* l) {
  __builtin_amdgcn_global_load_lds((const unsigned*)g, (unsigned*)l, 16, 0, 0);
}

template <int R, class RP>
__device__ __forceinline__ void stage_kc(RP rowptr, char* lds, int tid) {
#pragma unroll
  for (int i = 0; i < R / 32; ++i) {
    int row = i * 32 + (tid >> 3);
    int cp = tid & 7;
    int sc = cp ^ ((row >> 1) & 7);
    glds16(rowptr(row) + sc * 8, lds + (i * 256 + tid) * 16);
  }
}
__device__ __forceinline__ void stage_ks(const u16* g, long ld, char* lds, int tid) {
#pragma unroll
  for (int i = 0; i < 4; ++i) {
    int row = i * 16 + (tid >> 4);
    int cp = tid & 15;
    int sc = cp ^ (((row & 3) << 2) | ((row >> 2) & 3));
    glds16(g + (long)row * ld + sc * 8, lds + (i * 256 + tid) * 16);
  }
}
__device__ __forceinline__ bf16x8 frag_kc(const char* lds, int blk, int ks, int lane) {
  int row = blk * 32 + (lane & 31);
  int ch = ks * 2 + (lane >> 5);
  return *(const bf16x8*)(lds + row * 128 + ((ch ^ ((row >> 1) & 7)) << 4));
}
__device__ __forceinline__ bf16x8 frag_ks(const char* lds, int blk, int ks, int lane) {
  int hh = lane >> 5, b2 = (lane >> 4) & 1, q = (lane & 15) >> 2, p = lane & 3;
  bf16x8 r;
#pragma unroll
  for (int t = 0; t < 2; ++t) {
    int row = 16 * ks + 8 * hh + 4 * t + q;
    int ch = 4 * blk + 2 * b2 + (p >> 1);
    int off = 256 * row + 16 * (ch ^ (((row & 3) << 2) | ((row >> 2) & 3))) + 8 * (p & 1);
    bf16x4 v = __builtin_amdgcn_ds_read_tr16_b64_v4i16((lds_bf16x4*)(lds_cptr)(lds + off));
    r[4 * t + 0] = v[0]; r[4 * t + 1] = v[1]; r[4 * t + 2] = v[2]; r[4 * t + 3] = v[3];
  }
  return r;
}

#define MFMA32(a, b, c) __builtin_amdgcn_mfma_f32_32x32x16_bf16(a, b, c, 0, 0, 0)

#define K1A_BLOCKS 128
#define KSPLIT 4
#define K1B_BLOCKS (4 * 14 * KSPLIT)

__global__ __launch_bounds__(256, 2) void k_gemm1(const u16* __restrict__ xn, const u16* __restrict__ h,
                                                   const u16* __restrict__ Bre, const u16* __restrict__ Bim,
                                                   const u16* __restrict__ w1, float* __restrict__ R,
                                                   float* __restrict__ part) {
  __shared__ __attribute__((aligned(16))) char smem[2 * 32768];
  const int tid = threadIdx.x, lane = tid & 63, wid = tid >> 6, wm = wid >> 1, wn = wid & 1;
  f32x16 acc[2][2];
#pragma unroll
  for (int i = 0; i < 2; ++i)
#pragma unroll
    for (int j = 0; j < 2; ++j)
#pragma unroll
      for (int r = 0; r < 16; ++r) acc[i][j][r] = 0.f;

  if (blockIdx.x < K1A_BLOCKS) {
    const int mt = blockIdx.x >> 3, nt = blockIdx.x & 7;
    const int t0 = mt * 128, n0 = nt * 128;
    const int nk = DM / 64;
    auto stage = [&](int k, int buf) {
      char* a = smem + buf * 32768;
      char* b = a + 16384;
      stage_kc<128>([&](int r) { return xn + (long)(t0 + r) * DM + k * 64; }, a, tid);
      stage_kc<128>([&](int r) { int n = n0 + r; const u16* base = (n & 1) ? Bim : Bre; return base + (long)(n >> 1) * DM + k * 64; }, b, tid);
    };
    stage(0, 0);
    for (int k = 0; k < nk; ++k) {
      __syncthreads();
      if (k + 1 < nk) stage(k + 1, (k + 1) & 1);
      const char* a = smem + (k & 1) * 32768;
      const char* b = a + 16384;
#pragma unroll
      for (int ks = 0; ks < 4; ++ks) {
        bf16x8 fa[2], fb[2];
#pragma unroll
        for (int i = 0; i < 2; ++i) fa[i] = frag_kc(a, wm * 2 + i, ks, lane);
#pragma unroll
        for (int j = 0; j < 2; ++j) fb[j] = frag_kc(b, wn * 2 + j, ks, lane);
#pragma unroll
        for (int i = 0; i < 2; ++i)
#pragma unroll
          for (int j = 0; j < 2; ++j) acc[i][j] = MFMA32(fa[i], fb[j], acc[i][j]);
      }
    }
#pragma unroll
    for (int i = 0; i < 2; ++i)
#pragma unroll
      for (int j = 0; j < 2; ++j)
#pragma unroll
        for (int r = 0; r < 16; ++r) {
          int row = t0 + wm * 64 + i * 32 + (r & 3) + 8 * (r >> 2) + 4 * (lane >> 5);
          int col = n0 + wn * 64 + j * 32 + (lane & 31);
          R[(long)row * (2 * PP) + col] = acc[i][j][r];
        }
  } else {
    int b = blockIdx.x - K1A_BLOCKS;
    const int ksp = b & (KSPLIT - 1); b >>= 2;
    const int mt = b & 3, nt = b >> 2;
    const int d0 = mt * 128, f0 = nt * 128;
    const int kbeg = ksp * (SEQ / KSPLIT);
    const int nk = SEQ / KSPLIT / 64;
    auto stage = [&](int k, int buf) {
      char* a = smem + buf * 32768;
      char* bb = a + 16384;
      stage_ks(h + (long)(kbeg + k * 64) * DM + d0, DM, a, tid);
      stage_ks(w1 + (long)(kbeg + k * 64) * FF + f0, FF, bb, tid);
    };
    stage(0, 0);
    for (int k = 0; k < nk; ++k) {
      __syncthreads();
      if (k + 1 < nk) stage(k + 1, (k + 1) & 1);
      const char* a = smem + (k & 1) * 32768;
      const char* bb = a + 16384;
#pragma unroll
      for (int ks = 0; ks < 4; ++ks) {
        bf16x8 fa[2], fb[2];
#pragma unroll
        for (int i = 0; i < 2; ++i) fa[i] = frag_ks(a, wm * 2 + i, ks, lane);
#pragma unroll
        for (int j = 0; j < 2; ++j) fb[j] = frag_ks(bb, wn * 2 + j, ks, lane);
#pragma unroll
        for (int i = 0; i < 2; ++i)
#pragma unroll
          for (int j = 0; j < 2; ++j) acc[i][j] = MFMA32(fa[i], fb[j], acc[i][j]);
      }
    }
    float* po = part + (long)ksp * DM * FF;
#pragma unroll
    for (int i = 0; i < 2; ++i)
#pragma unroll
      for (int j = 0; j < 2; ++j)
#pragma unroll
        for (int r = 0; r < 16; ++r) {
          int row = d0 + wm * 64 + i * 32 + (r & 3) + 8 * (r >> 2) + 4 * (lane >> 5);
          int col = f0 + wn * 64 + j * 32 + (lane & 31);
          po[(long)row * FF + col] = acc[i][j][r];
        }
  }
}

#define SC_CH 8
#define SC_T 32
#define SC_NCHUNK (SEQ / SC_T)
#define SCAN_BLOCKS (PP / SC_CH)
#define RED_BLOCKS (DM * FF / (512 * 4))

__device__ __forceinline__ float gelu_tanh(float x) {
  const float k0 = 0.7978845608028654f, k1 = 0.044715f;
  float inner = k0 * (x + k1 * x * x * x);
  return 0.5f * x * (1.f + tanhf(inner));
}

__global__ __launch_bounds__(512) void k_scan(const float* __restrict__ R, const u16* __restrict__ Lre,
                                               const u16* __restrict__ Lim, const u16* __restrict__ logdt,
                                               u16* __restrict__ hseq, const float* __restrict__ part,
                                               const u16* __restrict__ b1, u16* __restrict__ u) {
  __shared__ float2 agg[SC_NCHUNK][SC_CH];
  if (blockIdx.x < SCAN_BLOCKS) {
    const int ch = threadIdx.x & (SC_CH - 1);
    const int chunk = threadIdx.x / SC_CH;
    const int p = blockIdx.x * SC_CH + ch;
    float dt = rbf(expf(bf2f(logdt[p])));
    float lr = bf2f(Lre[p]), li = bf2f(Lim[p]);
    float er = expf(dt * lr);
    float sn, cs;
    sincosf(dt * li, &sn, &cs);
    float ar = er * cs, ai = er * sn;
    float den = lr * lr + li * li;
    float nr = ar - 1.0f, ni = ai;
    float cr = (nr * lr + ni * li) / den, ci = (ni * lr - nr * li) / den;
    float hr[SC_T], hi[SC_T];
    const float2* Rp = (const float2*)(R + (long)chunk * SC_T * (2 * PP)) + p;
#pragma unroll
    for (int i = 0; i < SC_T; ++i) {
      float2 v = Rp[(long)i * PP];
      hr[i] = cr * v.x - ci * v.y;
      hi[i] = cr * v.y + ci * v.x;
    }
#pragma unroll
    for (int i = 1; i < SC_T; ++i) {
      float xr = ar * hr[i - 1] - ai * hi[i - 1] + hr[i];
      float xi = ar * hi[i - 1] + ai * hr[i - 1] + hi[i];
      hr[i] = xr; hi[i] = xi;
    }
    agg[chunk][ch] = make_float2(hr[SC_T - 1], hi[SC_T - 1]);
    float Ar = ar, Ai = ai;
#pragma unroll
    for (int s = 0; s < 5; ++s) { float tr = Ar * Ar - Ai * Ai, ti = 2.f * Ar * Ai; Ar = tr; Ai = ti; }
    __syncthreads();
    float gr = 0.f, gi = 0.f;
    for (int j = 0; j < chunk; ++j) {
      float2 a = agg[j][ch];
      float tr = Ar * gr - Ai * gi + a.x, ti = Ar * gi + Ai * gr + a.y;
      gr = tr; gi = ti;
    }
#pragma unroll
    for (int i = 0; i < SC_T; ++i) {
      float tr = ar * gr - ai * gi, ti = ar * gi + ai * gr;
      gr = tr; gi = ti;
      hr[i] += gr; hi[i] += gi;
    }
    u16* hp = hseq + (long)chunk * SC_T * (2 * PP) + p;
#pragma unroll
    for (int i = 0; i < SC_T; ++i) {
      hp[(long)i * (2 * PP)] = f2bf(hr[i]);
      hp[(long)i * (2 * PP) + PP] = f2bf(-hi[i]);
    }
  } else {
    long idx = ((long)(blockIdx.x - SCAN_BLOCKS) * 512 + threadIdx.x) * 4;
    f32x4 s = *(const f32x4*)(part + idx);
#pragma unroll
    for (int k = 1; k < KSPLIT; ++k) {
      f32x4 v = *(const f32x4*)(part + (long)k * DM * FF + idx);
      s += v;
    }
    int f = (int)(idx % FF);
    bf16x4 bb = *(const bf16x4*)(b1 + f);
    bf16x4 o;
#pragma unroll
    for (int j = 0; j < 4; ++j) {
      float v = rbf(rbf(s[j]) + bf2f((u16)bb[j]));
      o[j] = (short)f2bf(gelu_tanh(v));
    }
    *(bf16x4*)(u + idx) = o;
  }
}

__global__ __launch_bounds__(256, 2) void k_gemm3(const u16* __restrict__ hseq, const u16* __restrict__ Cre,
                                                   const u16* __restrict__ Cim, const u16* __restrict__ w2,
                                                   const u16* __restrict__ u, const u16* __restrict__ x,
                                                   const u16* __restrict__ xn, const u16* __restrict__ Dsk,
                                                   const u16* __restrict__ b2, float* __restrict__ out) {
  __shared__ __attribute__((aligned(16))) char smem[2 * 24576];
  const int tid = threadIdx.x, lane = tid & 63, wid = tid >> 6, wm = wid >> 1, wn = wid & 1;
  const int mt = blockIdx.x >> 3, nt = blockIdx.x & 7;
  const int s0 = mt * 128, d0 = nt * 64;
  f32x16 acc1[2], acc2[2];
#pragma unroll
  for (int i = 0; i < 2; ++i)
#pragma unroll
    for (int r = 0; r < 16; ++r) { acc1[i][r] = 0.f; acc2[i][r] = 0.f; }
  const int nk1 = (2 * PP) / 64, nk2 = FF / 64, nk = nk1 + nk2;
  auto stage = [&](int k, int buf) {
    char* a = smem + buf * 24576;
    char* b = a + 16384;
    if (k < nk1) {
      stage_kc<128>([&](int r) { return hseq + (long)(s0 + r) * (2 * PP) + k * 64; }, a, tid);
      const u16* cb = (k < nk1 / 2) ? Cre + k * 64 : Cim + (k - nk1 / 2) * 64;
      stage_kc<64>([&](int r) { return cb + (long)(d0 + r) * PP; }, b, tid);
    } else {
      int kk = k - nk1;
      stage_ks(w2 + (long)(kk * 64) * SEQ + s0, SEQ, a, tid);
      stage_kc<64>([&](int r) { return u + (long)(d0 + r) * FF + kk * 64; }, b, tid);
    }
  };
  stage(0, 0);
  for (int k = 0; k < nk; ++k) {
    __syncthreads();
    if (k + 1 < nk) stage(k + 1, (k + 1) & 1);
    const char* a = smem + (k & 1) * 24576;
    const char* b = a + 16384;
    if (k < nk1) {
#pragma unroll
      for (int ks = 0; ks < 4; ++ks) {
        bf16x8 fa[2], fb;
#pragma unroll
        for (int i = 0; i < 2; ++i) fa[i] = frag_kc(a, wm * 2 + i, ks, lane);
        fb = frag_kc(b, wn, ks, lane);
#pragma unroll
        for (int i = 0; i < 2; ++i) acc1[i] = MFMA32(fa[i], fb, acc1[i]);
      }
    } else {
#pragma unroll
      for (int ks = 0; ks < 4; ++ks) {
        bf16x8 fa[2], fb;
#pragma unroll
        for (int i = 0; i < 2; ++i) fa[i] = frag_ks(a, wm * 2 + i, ks, lane);
        fb = frag_kc(b, wn, ks, lane);
#pragma unroll
        for (int i = 0; i < 2; ++i) acc2[i] = MFMA32(fa[i], fb, acc2[i]);
      }
    }
  }
  const int col = d0 + wn * 32 + (lane & 31);
  const float dsk = bf2f(Dsk[col]);
#pragma unroll
  for (int i = 0; i < 2; ++i)
#pragma unroll
    for (int r = 0; r < 16; ++r) {
      int row = s0 + wm * 64 + i * 32 + (r & 3) + 8 * (r >> 2) + 4 * (lane >> 5);
      long o = (long)row * DM + col;
      float xv = bf2f(x[o]);
      float xnv = bf2f(xn[o]);
      float s = 2.0f * acc1[i][r] + rbf(xnv * dsk);
      float y2 = rbf(rbf(acc2[i][r]) + bf2f(b2[row]));
      float g = rbf(y2 / (1.0f + expf(-y2)));
      out[o] = xv + s * g;
    }
}

extern "C" void kernel_launch(void* const* d_in, const int* in_sizes, int n_in, void* d_out, int out_size,
                              void* d_ws, size_t ws_size, hipStream_t stream) {
  const u16* x = (const u16*)d_in[0];
  const u16* Lre = (const u16*)d_in[1];
  const u16* Lim = (const u16*)d_in[2];
  const u16* Bre = (const u16*)d_in[3];
  const u16* Bim = (const u16*)d_in[4];
  const u16* Cre = (const u16*)d_in[5];
  const u16* Cim = (const u16*)d_in[6];
  const u16* logdt = (const u16*)d_in[7];
  const u16* Dsk = (const u16*)d_in[8];
  const u16* w1 = (const u16*)d_in[10];
  const u16* b1 = (const u16*)d_in[11];
  const u16* w2 = (const u16*)d_in[12];
  const u16* b2 = (const u16*)d_in[13];
  float* out = (float*)d_out;

  char* ws = (char*)d_ws;
  u16* xn = (u16*)ws;                       ws += (size_t)SEQ * DM * 2;
  u16* h = (u16*)ws;                        ws += (size_t)SEQ * DM * 2;
  float* R = (float*)ws;                    ws += (size_t)SEQ * 2 * PP * 4;
  float* part = (float*)ws;                 ws += (size_t)KSPLIT * DM * FF * 4;
  u16* u = (u16*)ws;                        ws += (size_t)DM * FF * 2;
  u16* hseq = (u16*)ws;                     ws += (size_t)SEQ * 2 * PP * 2;

  k_ln<<<SEQ / 4, 256, 0, stream>>>(x, xn, h);
  k_gemm1<<<K1A_BLOCKS + K1B_BLOCKS, 256, 0, stream>>>(xn, h, Bre, Bim, w1, R, part);
  k_scan<<<SCAN_BLOCKS + RED_BLOCKS, 512, 0, stream>>>(R, Lre, Lim, logdt, hseq, part, b1, u);
  k_gemm3<<<(SEQ / 128) * (DM / 64), 256, 0, stream>>>(hseq, Cre, Cim, w2, u, x, xn, Dsk, b2, out);
}
```

```cpp
#include <hip/hip_runtime.h>
#include <hip/hip_bf16.h>

#define SEQ 2048
#define DM 512
#define PP 512
#define FF 1792

typedef unsigned short u16;
typedef __attribute__((ext_vector_type(8))) short bf16x8;
typedef __attribute__((ext_vector_type(4))) short bf16x4;
typedef __attribute__((ext_vector_type(16))) float f32x16;
typedef __attribute__((ext_vector_type(4))) float f32x4;
typedef __attribute__((address_space(3))) const char* lds_cptr;
typedef __attribute__((address_space(3))) bf16x4 lds_bf16x4;

__device__ __forceinline__ float bf2f(u16 v) { return __uint_as_float(((unsigned)v) << 16); }
__device__ __forceinline__ u16 f2bf(float f) {
  unsigned u = __float_as_uint(f);
  u += 0x7fffu + ((u >> 16) & 1u);
  return (u16)(u >> 16);
}
__device__ __forceinline__ float rbf(float f) { return bf2f(f2bf(f)); }

__device__ __forceinline__ float wave_sum(float v) {
#pragma unroll
  for (int o = 32; o > 0; o >>= 1) v += __shfl_xor(v, o);
  return v;
}

__device__ __forceinline__ void ln_bf16(const float (&in)[8], float (&out)[8]) {
  float s = 0.f;
#pragma unroll
  for (int j = 0; j < 8; ++j) s += in[j];
  s = wave_sum(s);
  float mean = s * (1.0f / DM);
  float q = 0.f;
#pragma unroll
  for (int j = 0; j < 8; ++j) { float d = in[j] - mean; q += d * d; }
  q = wave_sum(q);
  float var = rbf(q * (1.0f / DM));
  float m = rbf(mean);
  float sd = rbf(sqrtf(rbf(var + 1e-5f)));
#pragma unroll
  for (int j = 0; j < 8; ++j) out[j] = rbf(rbf(in[j] - m) / sd);
}

__global__ __launch_bounds__(256) void k_ln(const u16* __restrict__ x, u16* __restrict__ xn, u16* __restrict__ h) {
  int row = blockIdx.x * 4 + (threadIdx.x >> 6);
  int lane = threadIdx.x & 63;
  const u16* xr = x + (long)row * DM + lane * 8;
  bf16x8 v = *(const bf16x8*)xr;
  float in[8], o1[8], o2[8];
#pragma unroll
  for (int j = 0; j < 8; ++j) in[j] = bf2f((u16)v[j]);
  ln_bf16(in, o1);
  ln_bf16(o1, o2);
  bf16x8 w1, w2;
#pragma unroll
  for (int j = 0; j < 8; ++j) { w1[j] = (short)f2bf(o1[j]); w2[j] = (short)f2bf(o2[j]); }
  *(bf16x8*)(xn + (long)row * DM + lane * 8) = w1;
  *(bf16x8*)(h + (long)row * DM + lane * 8) = w2;
}

__device__ __forceinline__ void glds16(const void* g, unsigned lds_wave_base) {
  unsigned keep;
  asm volatile("s_mov_b32 %0, m0\n\ts_mov_b32 m0, %2\n\ts_nop 0\n\tglobal_load_lds_dwordx4 %1, off\n\ts_mov_b32 m0, %0"
               : "=&s"(keep) : "v"(g), "s"(lds_wave_base) : "memory");
}
__device__ __forceinline__ unsigned lds_addr(const void* p) { return (unsigned)(size_t)p; }
__device__ __forceinline__ unsigned uni(unsigned v) { return __builtin_amdgcn_readfirstlane(v); }

template <int R, class RP>
__device__ __forceinline__ void stage_kc(RP rowptr, unsigned lds, int tid) {
#pragma unroll
  for (int i = 0; i < R / 32; ++i) {
    int row = i * 32 + (tid >> 3);
    int cp = tid & 7;
    int sc = cp ^ ((row >> 1) & 7);
    glds16(rowptr(row) + sc * 8, uni(lds + (i * 256 + (tid & ~63)) * 16));
  }
}
__device__ __forceinline__ void stage_ks128(const u16* g, long ld, unsigned lds, int tid) {
#pragma unroll
  for (int i = 0; i < 4; ++i) {
    int row = i * 16 + (tid >> 4);
    int cp = tid & 15;
    int sc = cp ^ (((row & 3) << 2) | ((row >> 2) & 3));
    glds16(g + (long)row * ld + sc * 8, uni(lds + (i * 256 + (tid & ~63)) * 16));
  }
}
__device__ __forceinline__ void stage_ks64(const u16* g, long ld, unsigned lds, int tid) {
#pragma unroll
  for (int i = 0; i < 2; ++i) {
    int idx = i * 256 + tid;
    int l = idx & 63;
    int row = (idx >> 6) * 8 + ((l >> 2) & 7);
    int ch = 4 * (l >> 5) + ((l & 3) ^ ((row >> 2) & 3));
    glds16(g + (long)row * ld + ch * 8, uni(lds + (idx & ~63) * 16));
  }
}
__device__ __forceinline__ bf16x8 frag_kc(const char* lds, int blk, int ks, int lane) {
  int row = blk * 32 + (lane & 31);
  int ch = ks * 2 + (lane >> 5);
  return *(const bf16x8*)(lds + row * 128 + ((ch ^ ((row >> 1) & 7)) << 4));
}
__device__ __forceinline__ bf16x8 frag_ks128(const char* lds, int blk, int ks, int lane) {
  int hh = lane >> 5, b2 = (lane >> 4) & 1, q = (lane & 15) >> 2, p = lane & 3;
  bf16x8 r;
#pragma unroll
  for (int t = 0; t < 2; ++t) {
    int row = 16 * ks + 8 * hh + 4 * t + q;
    int ch = 4 * blk + 2 * b2 + (p >> 1);
    int off = 256 * row + 16 * (ch ^ (((row & 3) << 2) | ((row >> 2) & 3))) + 8 * (p & 1);
    bf16x4 v = __builtin_amdgcn_ds_read_tr16_b64_v4i16((lds_bf16x4*)(lds_cptr)(lds + off));
    r[4 * t + 0] = v[0]; r[4 * t + 1] = v[1]; r[4 * t + 2] = v[2]; r[4 * t + 3] = v[3];
  }
  return r;
}
__device__ __forceinline__ bf16x8 frag_ks64(const char* lds, int blk, int ks, int lane) {
  int hh = lane >> 5, b2 = (lane >> 4) & 1, q = (lane & 15) >> 2, p = lane & 3;
  bf16x8 r;
#pragma unroll
  for (int t = 0; t < 2; ++t) {
    int row = 16 * ks + 8 * hh + 4 * t + q;
    int ch = 4 * blk + 2 * b2 + (p >> 1);
    int off = 1024 * (row >> 3) + 512 * (ch >> 2) + 64 * (row & 7) + 16 * ((ch & 3) ^ ((row >> 2) & 3)) + 8 * (p & 1);
    bf16x4 v = __builtin_amdgcn_ds_read_tr16_b64_v4i16((lds_bf16x4*)(lds_cptr)(lds + off));
    r[4 * t + 0] = v[0]; r[4 * t + 1] = v[1]; r[4 * t + 2] = v[2]; r[4 * t + 3] = v[3];
  }
  return r;
}

#define MFMA32(a, b, c) __builtin_amdgcn_mfma_f32_32x32x16_bf16(a, b, c, 0, 0, 0)

template <int NST, int LPS, class StageF, class ComputeF>
__device__ __forceinline__ void pipeline(int nk, StageF stage, ComputeF compute) {
#pragma unroll
  for (int s = 0; s < NST - 1; ++s)
    if (s < nk) stage(s, s);
  for (int k = 0; k < nk; ++k) {
    if (k + NST - 2 < nk) {
      asm volatile("s_waitcnt vmcnt(%0)" ::"n"((NST - 2) * LPS) : "memory");
    } else {
      asm volatile("s_waitcnt vmcnt(0)" ::: "memory");
    }
    __builtin_amdgcn_s_barrier();
    if (k + NST - 1 < nk) stage(k + NST - 1, (k + NST - 1) % NST);
    compute(k, k % NST);
  }
}

#define KSPLIT 4
#define K1_BLOCKS (128 + 4 * 14 * KSPLIT)
#define K1_NST 2

__global__ __launch_bounds__(256, 2) void k_gemm1(const u16* __restrict__ xn, const u16* __restrict__ h,
                                                   const u16* __restrict__ Bre, const u16* __restrict__ Bim,
                                                   const u16* __restrict__ w1, float* __restrict__ Rt,
                                                   float* __restrict__ part) {
  __shared__ __attribute__((aligned(16))) char smem[K1_NST * 32768];
  const int tid = threadIdx.x, lane = tid & 63, wid = tid >> 6, wm = wid >> 1, wn = wid & 1;
  f32x16 acc[2][2];
#pragma unroll
  for (int i = 0; i < 2; ++i)
#pragma unroll
    for (int j = 0; j < 2; ++j)
#pragma unroll
      for (int r = 0; r < 16; ++r) acc[i][j][r] = 0.f;

  const int xcd = blockIdx.x & 7, loc = blockIdx.x >> 3;
  if (loc >= 28) {
    const int l = loc - 28;
    const int mt = l & 7, nt = xcd * 2 + (l >> 3);
    const int m0 = mt * 128, t0 = nt * 128;
    const u16* bbase = (mt < 4) ? Bre + (long)m0 * DM : Bim + (long)(m0 - PP) * DM;
    auto stage = [&](int k, int buf) {
      unsigned a = lds_addr(smem) + buf * 32768;
      unsigned b = a + 16384;
      stage_kc<128>([&](int r) { return bbase + (long)r * DM + k * 64; }, a, tid);
      stage_kc<128>([&](int r) { return xn + (long)(t0 + r) * DM + k * 64; }, b, tid);
    };
    auto compute = [&](int k, int buf) {
      const char* a = smem + buf * 32768;
      const char* b = a + 16384;
#pragma unroll
      for (int ks = 0; ks < 4; ++ks) {
        bf16x8 fa[2], fb[2];
#pragma unroll
        for (int i = 0; i < 2; ++i) fa[i] = frag_kc(a, wm * 2 + i, ks, lane);
#pragma unroll
        for (int j = 0; j < 2; ++j) fb[j] = frag_kc(b, wn * 2 + j, ks, lane);
#pragma unroll
        for (int i = 0; i < 2; ++i)
#pragma unroll
          for (int j = 0; j < 2; ++j) acc[i][j] = MFMA32(fa[i], fb[j], acc[i][j]);
      }
    };
    pipeline<K1_NST, 8>(DM / 64, stage, compute);
#pragma unroll
    for (int i = 0; i < 2; ++i)
#pragma unroll
      for (int j = 0; j < 2; ++j)
#pragma unroll
        for (int r = 0; r < 16; ++r) {
          int row = m0 + wm * 64 + i * 32 + (r & 3) + 8 * (r >> 2) + 4 * (lane >> 5);
          int col = t0 + wn * 64 + j * 32 + (lane & 31);
          Rt[(long)row * SEQ + col] = acc[i][j][r];
        }
  } else {
    const int ksp = xcd >> 1;
    const int mt = (xcd & 1) * 2 + (loc & 1), nt = loc >> 1;
    const int d0 = mt * 128, f0 = nt * 128;
    const int kbeg = ksp * (SEQ / KSPLIT);
    auto stage = [&](int k, int buf) {
      unsigned a = lds_addr(smem) + buf * 32768;
      unsigned bb = a + 16384;
      stage_ks128(h + (long)(kbeg + k * 64) * DM + d0, DM, a, tid);
      stage_ks128(w1 + (long)(kbeg + k * 64) * FF + f0, FF, bb, tid);
    };
    auto compute = [&](int k, int buf) {
      const char* a = smem + buf * 32768;
      const char* bb = a + 16384;
#pragma unroll
      for (int ks = 0; ks < 4; ++ks) {
        bf16x8 fa[2], fb[2];
#pragma unroll
        for (int i = 0; i < 2; ++i) fa[i] = frag_ks128(a, wm * 2 + i, ks, lane);
#pragma unroll
        for (int j = 0; j < 2; ++j) fb[j] = frag_ks128(bb, wn * 2 + j, ks, lane);
#pragma unroll
        for (int i = 0; i < 2; ++i)
#pragma unroll
          for (int j = 0; j < 2; ++j) acc[i][j] = MFMA32(fa[i], fb[j], acc[i][j]);
      }
    };
    pipeline<K1_NST, 8>(SEQ / KSPLIT / 64, stage, compute);
    float* po = part + (long)ksp * DM * FF;
#pragma unroll
    for (int i = 0; i < 2; ++i)
#pragma unroll
      for (int j = 0; j < 2; ++j)
#pragma unroll
        for (int r = 0; r < 16; ++r) {
          int row = d0 + wm * 64 + i * 32 + (r & 3) + 8 * (r >> 2) + 4 * (lane >> 5);
          int col = f0 + wn * 64 + j * 32 + (lane & 31);
          po[(long)row * FF + col] = acc[i][j][r];
        }
  }
}

#define SCAN_BLOCKS PP
#define RED_BLOCKS (DM * FF / (256 * 4))

__device__ __forceinline__ float gelu_tanh(float x) {
  const float k0 = 0.7978845608028654f, k1 = 0.044715f;
  float inner = k0 * (x + k1 * x * x * x);
  return 0.5f * x * (1.f + tanhf(inner));
}

__global__ __launch_bounds__(256) void k_scan(const float* __restrict__ Rt, const u16* __restrict__ Lre,
                                               const u16* __restrict__ Lim, const u16* __restrict__ logdt,
                                               u16* __restrict__ hsT, const float* __restrict__ part,
                                               const u16* __restrict__ b1, u16* __restrict__ u) {
  __shared__ float2 wtot[4];
  if (blockIdx.x < SCAN_BLOCKS) {
    const int p = blockIdx.x;
    const int tid = threadIdx.x, lane = tid & 63, wid = tid >> 6;
    const float* pr = Rt + (long)p * SEQ + tid * 8;
    const float* pi = Rt + (long)(PP + p) * SEQ + tid * 8;
    f32x4 vr0 = *(const f32x4*)pr, vr1 = *(const f32x4*)(pr + 4);
    f32x4 vi0 = *(const f32x4*)pi, vi1 = *(const f32x4*)(pi + 4);
    float dt = rbf(expf(bf2f(logdt[p])));
    float lr = bf2f(Lre[p]), li = bf2f(Lim[p]);
    float er = expf(dt * lr);
    float sn, cs;
    sincosf(dt * li, &sn, &cs);
    float ar = er * cs, ai = er * sn;
    float den = lr * lr + li * li;
    float nr = ar - 1.0f, ni = ai;
    float cr = (nr * lr + ni * li) / den, ci = (ni * lr - nr * li) / den;
    float hr[8], hi[8];
#pragma unroll
    for (int i = 0; i < 8; ++i) {
      float x = i < 4 ? vr0[i & 3] : vr1[i & 3];
      float y = i < 4 ? vi0[i & 3] : vi1[i & 3];
      hr[i] = cr * x - ci * y;
      hi[i] = cr * y + ci * x;
    }
#pragma unroll
    for (int i = 1; i < 8; ++i) {
      float xr = ar * hr[i - 1] - ai * hi[i - 1] + hr[i];
      float xi = ar * hi[i - 1] + ai * hr[i - 1] + hi[i];
      hr[i] = xr; hi[i] = xi;
    }
    float Mr = ar, Mi = ai;
#pragma unroll
    for (int s = 0; s < 3; ++s) { float tr = Mr * Mr - Mi * Mi, ti = 2.f * Mr * Mi; Mr = tr; Mi = ti; }
    float vr = hr[7], vi = hi[7];
    float Lr = 1.f, Li = 0.f;
#pragma unroll
    for (int s = 0; s < 6; ++s) {
      int o = 1 << s;
      float qr = __shfl_up(vr, o), qi = __shfl_up(vi, o);
      if (lane >= o) {
        vr += Mr * qr - Mi * qi;
        vi += Mr * qi + Mi * qr;
      }
      if ((lane >> s) & 1) { float tr = Lr * Mr - Li * Mi, ti = Lr * Mi + Li * Mr; Lr = tr; Li = ti; }
      float tr = Mr * Mr - Mi * Mi, ti = 2.f * Mr * Mi; Mr = tr; Mi = ti;
    }
    if (lane == 63) wtot[wid] = make_float2(vr, vi);
    float er_ = __shfl_up(vr, 1), ei_ = __shfl_up(vi, 1);
    if (lane == 0) { er_ = 0.f; ei_ = 0.f; }
    __syncthreads();
    float c_r = 0.f, c_i = 0.f;
#pragma unroll
    for (int w = 0; w < 3; ++w) {
      if (w < wid) {
        float2 t = wtot[w];
        float tr = Mr * c_r - Mi * c_i + t.x, ti = Mr * c_i + Mi * c_r + t.y;
        c_r = tr; c_i = ti;
      }
    }
    float gr = er_ + Lr * c_r - Li * c_i, gi = ei_ + Lr * c_i + Li * c_r;
    bf16x8 ore, oim;
#pragma unroll
    for (int i = 0; i < 8; ++i) {
      float tr = ar * gr - ai * gi, ti = ar * gi + ai * gr;
      gr = tr; gi = ti;
      ore[i] = (short)f2bf(hr[i] + gr);
      oim[i] = (short)f2bf(-(hi[i] + gi));
    }
    *(bf16x8*)(hsT + (long)p * SEQ + tid * 8) = ore;
    *(bf16x8*)(hsT + (long)(PP + p) * SEQ + tid * 8) = oim;
  } else {
    long idx = ((long)(blockIdx.x - SCAN_BLOCKS) * 256 + threadIdx.x) * 4;
    f32x4 s = *(const f32x4*)(part + idx);
#pragma unroll
    for (int k = 1; k < KSPLIT; ++k) {
      f32x4 v = *(const f32x4*)(part + (long)k * DM * FF + idx);
      s += v;
    }
    int f = (int)(idx % FF);
    bf16x4 bb = *(const bf16x4*)(b1 + f);
    bf16x4 o;
#pragma unroll
    for (int j = 0; j < 4; ++j) {
      float v = rbf(rbf(s[j]) + bf2f((u16)bb[j]));
      o[j] = (short)f2bf(gelu_tanh(v));
    }
    *(bf16x4*)(u + idx) = o;
  }
}

#define K3_NST 3
#define K3_STAGE 32768
__global__ __launch_bounds__(256, 1) void k_gemm3(const u16* __restrict__ hsT, const u16* __restrict__ Cre,
                                                   const u16* __restrict__ Cim, const u16* __restrict__ w2,
                                                   const u16* __restrict__ u, const u16* __restrict__ x,
                                                   const u16* __restrict__ xn, const u16* __restrict__ Dsk,
                                                   const u16* __restrict__ b2, float* __restrict__ out) {
  __shared__ __attribute__((aligned(16))) char smem[K3_NST * K3_STAGE];
  const int tid = threadIdx.x, lane = tid & 63, wid = tid >> 6;
  const int xcd = blockIdx.x & 7, loc = blockIdx.x >> 3;
  const int mt = xcd * 4 + (loc & 3), nt = loc >> 2;
  const int s0 = mt * 64, d0 = nt * 64;
  f32x16 acc1[2][2], acc2[2][2];
#pragma unroll
  for (int i = 0; i < 2; ++i)
#pragma unroll
    for (int j = 0; j < 2; ++j)
#pragma unroll
      for (int r = 0; r < 16; ++r) { acc1[i][j][r] = 0.f; acc2[i][j][r] = 0.f; }
  constexpr int nk1 = (2 * PP) / 128, nk2 = FF / 128, nk = nk1 + nk2;
  auto stage = [&](int k, int buf) {
#pragma unroll
    for (int hf = 0; hf < 2; ++hf) {
      unsigned a = lds_addr(smem) + buf * K3_STAGE + hf * 16384;
      unsigned b = a + 8192;
      if (k < nk1) {
        int k64 = k * 2 + hf;
        stage_ks64(hsT + (long)(k64 * 64) * SEQ + s0, SEQ, a, tid);
        const u16* cb = (k < nk1 / 2) ? Cre + k64 * 64 : Cim + (k64 - nk1) * 64;
        stage_kc<64>([&](int r) { return cb + (long)(d0 + r) * PP; }, b, tid);
      } else {
        int k64 = (k - nk1) * 2 + hf;
        stage_ks64(w2 + (long)(k64 * 64) * SEQ + s0, SEQ, a, tid);
        stage_kc<64>([&](int r) { return u + (long)(d0 + r) * FF + k64 * 64; }, b, tid);
      }
    }
  };
  auto compute = [&](int k, int buf) {
    bf16x8 fa[2][2], fb[2][2];
#pragma unroll
    for (int hf = 0; hf < 2; ++hf) {
      const char* a = smem + buf * K3_STAGE + hf * 16384;
      const char* b = a + 8192;
#pragma unroll
      for (int i = 0; i < 2; ++i) fa[hf][i] = frag_ks64(a, i, wid, lane);
#pragma unroll
      for (int j = 0; j < 2; ++j) fb[hf][j] = frag_kc(b, j, wid, lane);
    }
    if (k < nk1) {
#pragma unroll
      for (int hf = 0; hf < 2; ++hf)
#pragma unroll
        for (int i = 0; i < 2; ++i)
#pragma unroll
          for (int j = 0; j < 2; ++j) acc1[i][j] = MFMA32(fa[hf][i], fb[hf][j], acc1[i][j]);
    } else {
#pragma unroll
      for (int hf = 0; hf < 2; ++hf)
#pragma unroll
        for (int i = 0; i < 2; ++i)
#pragma unroll
          for (int j = 0; j < 2; ++j) acc2[i][j] = MFMA32(fa[hf][i], fb[hf][j], acc2[i][j]);
    }
  };
  pipeline<K3_NST, 8>(nk, stage, compute);

  float* red = (float*)smem;
  const int orow = tid >> 2, ocol = (tid & 3) * 16;
  float s1[16], s2[16];
#pragma unroll
  for (int pass = 0; pass < 2; ++pass) {
    __syncthreads();
#pragma unroll
    for (int i = 0; i < 2; ++i)
#pragma unroll
      for (int j = 0; j < 2; ++j)
#pragma unroll
        for (int r = 0; r < 16; ++r) {
          int row = i * 32 + (r & 3) + 8 * (r >> 2) + 4 * (lane >> 5);
          int col = j * 32 + (lane & 31);
          red[wid * 4096 + row * 64 + col] = pass == 0 ? acc1[i][j][r] : acc2[i][j][r];
        }
    __syncthreads();
#pragma unroll
    for (int c4 = 0; c4 < 4; ++c4) {
      f32x4 t = *(const f32x4*)(red + orow * 64 + ocol + c4 * 4);
#pragma unroll
      for (int w = 1; w < 4; ++w) t += *(const f32x4*)(red + w * 4096 + orow * 64 + ocol + c4 * 4);
#pragma unroll
      for (int e = 0; e < 4; ++e) { if (pass == 0) s1[c4 * 4 + e] = t[e]; else s2[c4 * 4 + e] = t[e]; }
    }
  }
  const int row = s0 + orow;
  const long o = (long)row * DM + d0 + ocol;
  const float b2v = bf2f(b2[row]);
  bf16x8 xv0 = *(const bf16x8*)(x + o), xv1 = *(const bf16x8*)(x + o + 8);
  bf16x8 xnv0 = *(const bf16x8*)(xn + o), xnv1 = *(const bf16x8*)(xn + o + 8);
  bf16x8 dk0 = *(const bf16x8*)(Dsk + d0 + ocol), dk1 = *(const bf16x8*)(Dsk + d0 + ocol + 8);
  float res[16];
#pragma unroll
  for (int e = 0; e < 16; ++e) {
    float xv = bf2f((u16)(e < 8 ? xv0[e & 7] : xv1[e & 7]));
    float xnv = bf2f((u16)(e < 8 ? xnv0[e & 7] : xnv1[e & 7]));
    float dk = bf2f((u16)(e < 8 ? dk0[e & 7] : dk1[e & 7]));
    float s = 2.0f * s1[e] + rbf(xnv * dk);
    float y2 = rbf(rbf(s2[e]) + b2v);
    float g = rbf(y2 / (1.0f + expf(-y2)));
    res[e] = xv + s * g;
  }
#pragma unroll
  for (int c4 = 0; c4 < 4; ++c4)
    *(f32x4*)(out + o + c4 * 4) = (f32x4){res[c4 * 4], res[c4 * 4 + 1], res[c4 * 4 + 2], res[c4 * 4 + 3]};
}

extern "C" void kernel_launch(void* const* d_in, const int* in_sizes, int n_in, void* d_out, int out_size,
                              void* d_ws, size_t ws_size, hipStream_t stream) {
  const u16* x = (const u16*)d_in[0];
  const u16* Lre = (const u16*)d_in[1];
  const u16* Lim = (const u16*)d_in[2];
  const u16* Bre = (const u16*)d_in[3];
  const u16* Bim = (const u16*)d_in[4];
  const u16* Cre = (const u16*)d_in[5];
  const u16* Cim = (const u16*)d_in[6];
  const u16* logdt = (const u16*)d_in[7];
  const u16* Dsk = (const u16*)d_in[8];
  const u16* w1 = (const u16*)d_in[10];
  const u16* b1 = (const u16*)d_in[11];
  const u16* w2 = (const u16*)d_in[12];
  const u16* b2 = (const u16*)d_in[13];
  float* out = (float*)d_out;

  char* ws = (char*)d_ws;
  u16* xn = (u16*)ws;                       ws += (size_t)SEQ * DM * 2;
  u16* h = (u16*)ws;                        ws += (size_t)SEQ * DM * 2;
  float* Rt = (float*)ws;                   ws += (size_t)SEQ * 2 * PP * 4;
  float* part = (float*)ws;                 ws += (size_t)KSPLIT * DM * FF * 4;
  u16* u = (u16*)ws;                        ws += (size_t)DM * FF * 2;
  u16* hsT = (u16*)ws;                      ws += (size_t)SEQ * 2 * PP * 2;

  k_ln<<<SEQ / 4, 256, 0, stream>>>(x, xn, h);
  k_gemm1<<<K1_BLOCKS, 256, 0, stream>>>(xn, h, Bre, Bim, w1, Rt, part);
  k_scan<<<SCAN_BLOCKS + RED_BLOCKS, 256, 0, stream>>>(Rt, Lre, Lim, logdt, hsT, part, b1, u);
  k_gemm3<<<256, 256, 0, stream>>>(hsT, Cre, Cim, w2, u, x, xn, Dsk, b2, out);
}
```

```cpp
#include <hip/hip_runtime.h>
#include <hip/hip_bf16.h>

#define SEQ 2048
#define DM 512
#define PP 512
#define FF 1792

typedef unsigned short u16;
typedef __attribute__((ext_vector_type(8))) short bf16x8;
typedef __attribute__((ext_vector_type(4))) short bf16x4;
typedef __attribute__((ext_vector_type(16))) float f32x16;
typedef __attribute__((ext_vector_type(4))) float f32x4;
typedef __attribute__((address_space(3))) const char* lds_cptr;
typedef __attribute__((address_space(3))) bf16x4 lds_bf16x4;

__device__ __forceinline__ float bf2f(u16 v) { return __uint_as_float(((unsigned)v) << 16); }
__device__ __forceinline__ u16 f2bf(float f) {
  unsigned u = __float_as_uint(f);
  u += 0x7fffu + ((u >> 16) & 1u);
  return (u16)(u >> 16);
}
__device__ __forceinline__ float rbf(float f) { return bf2f(f2bf(f)); }

__device__ __forceinline__ float wave_sum(float v) {
#pragma unroll
  for (int o = 32; o > 0; o >>= 1) v += __shfl_xor(v, o);
  return v;
}

__device__ __forceinline__ void ln_bf16(const float (&in)[8], float (&out)[8]) {
  float s = 0.f;
#pragma unroll
  for (int j = 0; j < 8; ++j) s += in[j];
  s = wave_sum(s);
  float mean = s * (1.0f / DM);
  float q = 0.f;
#pragma unroll
  for (int j = 0; j < 8; ++j) { float d = in[j] - mean; q += d * d; }
  q = wave_sum(q);
  float var = rbf(q * (1.0f / DM));
  float m = rbf(mean);
  float sd = rbf(sqrtf(rbf(var + 1e-5f)));
#pragma unroll
  for (int j = 0; j < 8; ++j) out[j] = rbf(rbf(in[j] - m) / sd);
}

__global__ __launch_bounds__(256) void k_ln(const u16* __restrict__ x, u16* __restrict__ xn, u16* __restrict__ h) {
  int row = blockIdx.x * 4 + (threadIdx.x >> 6);
  int lane = threadIdx.x & 63;
  const u16* xr = x + (long)row * DM + lane * 8;
  bf16x8 v = *(const bf16x8*)xr;
  float in[8], o1[8], o2[8];
#pragma unroll
  for (int j = 0; j < 8; ++j) in[j] = bf2f((u16)v[j]);
  ln_bf16(in, o1);
  ln_bf16(o1, o2);
  bf16x8 w1, w2;
#pragma unroll
  for (int j = 0; j < 8; ++j) { w1[j] = (short)f2bf(o1[j]); w2[j] = (short)f2bf(o2[j]); }
  *(bf16x8*)(xn + (long)row * DM + lane * 8) = w1;
  *(bf16x8*)(h + (long)row * DM + lane * 8) = w2;
}

__device__ __forceinline__ void glds16(const void* sbase, unsigned voff, unsigned lds_wave_base) {
  unsigned keep;
  asm volatile("s_mov_b32 %0, m0\n\ts_mov_b32 m0, %3\n\ts_nop 0\n\tglobal_load_lds_dwordx4 %1, %2\n\ts_mov_b32 m0, %0"
               : "=&s"(keep) : "v"(voff), "s"(sbase), "s"(lds_wave_base) : "memory");
}
__device__ __forceinline__ unsigned lds_addr(const void* p) { return (unsigned)(size_t)p; }
__device__ __forceinline__ unsigned uni(unsigned v) { return __builtin_amdgcn_readfirstlane(v); }
__device__ __forceinline__ const void* uni_ptr(const void* p) {
  unsigned long long v = (unsigned long long)p;
  unsigned lo = __builtin_amdgcn_readfirstlane((unsigned)v), hi = __builtin_amdgcn_readfirstlane((unsigned)(v >> 32));
  return (const void*)(((unsigned long long)hi << 32) | lo);
}

__device__ __forceinline__ int kc_row(int slot) { return slot >> 3; }
__device__ __forceinline__ int kc_col(int slot) { int row = slot >> 3; return ((slot & 7) ^ ((row >> 1) & 7)) * 8; }
__device__ __forceinline__ int ks128_row(int slot) { return slot >> 4; }
__device__ __forceinline__ int ks128_col(int slot) { int row = slot >> 4; return ((slot & 15) ^ (((row & 3) << 2) | ((row >> 2) & 3))) * 8; }
__device__ __forceinline__ int ks64_row(int slot) { return (slot >> 6) * 8 + ((slot >> 2) & 7); }
__device__ __forceinline__ int ks64_col(int slot) { int row = ks64_row(slot); return (4 * ((slot >> 5) & 1) + ((slot & 3) ^ ((row >> 2) & 3))) * 8; }
__device__ __forceinline__ void put(const void* sbase, unsigned voff, unsigned tile, int slot) { glds16(uni_ptr(sbase), voff, uni(tile + (slot & ~63) * 16)); }

__device__ __forceinline__ bf16x8 frag_kc(const char* lds, int blk, int ks, int lane) {
  int row = blk * 32 + (lane & 31);
  int ch = ks * 2 + (lane >> 5);
  return *(const bf16x8*)(lds + row * 128 + ((ch ^ ((row >> 1) & 7)) << 4));
}
__device__ __forceinline__ bf16x8 frag_ks128(const char* lds, int blk, int ks, int lane) {
  int hh = lane >> 5, b2 = (lane >> 4) & 1, q = (lane & 15) >> 2, p = lane & 3;
  bf16x8 r;
#pragma unroll
  for (int t = 0; t < 2; ++t) {
    int row = 16 * ks + 8 * hh + 4 * t + q;
    int ch = 4 * blk + 2 * b2 + (p >> 1);
    int off = 256 * row + 16 * (ch ^ (((row & 3) << 2) | ((row >> 2) & 3))) + 8 * (p & 1);
    bf16x4 v = __builtin_amdgcn_ds_read_tr16_b64_v4i16((lds_bf16x4*)(lds_cptr)(lds + off));
    r[4 * t + 0] = v[0]; r[4 * t + 1] = v[1]; r[4 * t + 2] = v[2]; r[4 * t + 3] = v[3];
  }
  return r;
}
__device__ __forceinline__ bf16x8 frag_ks64(const char* lds, int blk, int ks, int lane) {
  int hh = lane >> 5, b2 = (lane >> 4) & 1, q = (lane & 15) >> 2, p = lane & 3;
  bf16x8 r;
#pragma unroll
  for (int t = 0; t < 2; ++t) {
    int row = 16 * ks + 8 * hh + 4 * t + q;
    int ch = 4 * blk + 2 * b2 + (p >> 1);
    int off = 1024 * (row >> 3) + 512 * (ch >> 2) + 64 * (row & 7) + 16 * ((ch & 3) ^ ((row >> 2) & 3)) + 8 * (p & 1);
    bf16x4 v = __builtin_amdgcn_ds_read_tr16_b64_v4i16((lds_bf16x4*)(lds_cptr)(lds + off));
    r[4 * t + 0] = v[0]; r[4 * t + 1] = v[1]; r[4 * t + 2] = v[2]; r[4 * t + 3] = v[3];
  }
  return r;
}

#define MFMA32(a, b, c) __builtin_amdgcn_mfma_f32_32x32x16_bf16(a, b, c, 0, 0, 0)

template <int NST, int LPS, class StageF, class ComputeF>
__device__ __forceinline__ void pipeline(int nk, StageF stage, ComputeF compute) {
#pragma unroll
  for (int s = 0; s < NST - 1; ++s)
    if (s < nk) stage(s, s);
  for (int k = 0; k < nk; ++k) {
    if (k + NST - 2 < nk) {
      asm volatile("s_waitcnt vmcnt(%0)" ::"n"((NST - 2) * LPS) : "memory");
    } else {
      asm volatile("s_waitcnt vmcnt(0)" ::: "memory");
    }
    __builtin_amdgcn_s_barrier();
    if (k + NST - 1 < nk) stage(k + NST - 1, (k + NST - 1) % NST);
    compute(k, k % NST);
  }
}

#define KSPLIT 4
#define K1_BLOCKS (128 + 4 * 14 * KSPLIT)
#define K1_NST 2
#define K1_NT 512

__global__ __launch_bounds__(K1_NT, 4) void k_gemm1(const u16* __restrict__ xn, const u16* __restrict__ h,
                                                   const u16* __restrict__ Bre, const u16* __restrict__ Bim,
                                                   const u16* __restrict__ w1, float* __restrict__ Rt,
                                                   float* __restrict__ part) {
  __shared__ __attribute__((aligned(16))) char smem[K1_NST * 32768];
  const int tid = threadIdx.x, lane = tid & 63, wid = tid >> 6, wm = wid >> 2, wn = wid & 3;
  f32x16 acc[2];
#pragma unroll
  for (int i = 0; i < 2; ++i)
#pragma unroll
    for (int r = 0; r < 16; ++r) acc[i][r] = 0.f;

  const int xcd = blockIdx.x & 7, loc = blockIdx.x >> 3;
  if (loc >= 28) {
    const int l = loc - 28;
    const int mt = l & 7, nt = xcd * 2 + (l >> 3);
    const int m0 = mt * 128, t0 = nt * 128;
    const u16* bbase = (mt < 4) ? Bre + (long)m0 * DM : Bim + (long)(m0 - PP) * DM;
    auto stage = [&](int k, int buf) {
      unsigned a = lds_addr(smem) + buf * 32768;
      unsigned b = a + 16384;
#pragma unroll
      for (int i = 0; i < 2; ++i) {
        int slot = i * K1_NT + tid;
        unsigned vo = (kc_row(slot) * DM + kc_col(slot)) * 2;
        put(bbase + k * 64, vo, a, slot);
        put(xn + (long)t0 * DM + k * 64, vo, b, slot);
      }
    };
    auto compute = [&](int k, int buf) {
      const char* a = smem + buf * 32768;
      const char* b = a + 16384;
#pragma unroll
      for (int ks = 0; ks < 4; ++ks) {
        bf16x8 fa[2], fb;
#pragma unroll
        for (int i = 0; i < 2; ++i) fa[i] = frag_kc(a, wm * 2 + i, ks, lane);
        fb = frag_kc(b, wn, ks, lane);
#pragma unroll
        for (int i = 0; i < 2; ++i) acc[i] = MFMA32(fa[i], fb, acc[i]);
      }
    };
    pipeline<K1_NST, 4>(DM / 64, stage, compute);
#pragma unroll
    for (int i = 0; i < 2; ++i)
#pragma unroll
      for (int r = 0; r < 16; ++r) {
        int row = m0 + wm * 64 + i * 32 + (r & 3) + 8 * (r >> 2) + 4 * (lane >> 5);
        int col = t0 + wn * 32 + (lane & 31);
        Rt[(long)row * SEQ + col] = acc[i][r];
      }
  } else {
    const int ksp = xcd >> 1;
    const int mt = (xcd & 1) * 2 + (loc & 1), nt = loc >> 1;
    const int d0 = mt * 128, f0 = nt * 128;
    const int kbeg = ksp * (SEQ / KSPLIT);
    auto stage = [&](int k, int buf) {
      unsigned a = lds_addr(smem) + buf * 32768;
      unsigned bb = a + 16384;
#pragma unroll
      for (int i = 0; i < 2; ++i) {
        int slot = i * K1_NT + tid;
        put(h + (long)(kbeg + k * 64) * DM + d0, (ks128_row(slot) * DM + ks128_col(slot)) * 2, a, slot);
        put(w1 + (long)(kbeg + k * 64) * FF + f0, (ks128_row(slot) * FF + ks128_col(slot)) * 2, bb, slot);
      }
    };
    auto compute = [&](int k, int buf) {
      const char* a = smem + buf * 32768;
      const char* bb = a + 16384;
#pragma unroll
      for (int ks = 0; ks < 4; ++ks) {
        bf16x8 fa[2], fb;
#pragma unroll
        for (int i = 0; i < 2; ++i) fa[i] = frag_ks128(a, wm * 2 + i, ks, lane);
        fb = frag_ks128(bb, wn, ks, lane);
#pragma unroll
        for (int i = 0; i < 2; ++i) acc[i] = MFMA32(fa[i], fb, acc[i]);
      }
    };
    pipeline<K1_NST, 4>(SEQ / KSPLIT / 64, stage, compute);
    float* po = part + (long)ksp * DM * FF;
#pragma unroll
    for (int i = 0; i < 2; ++i)
#pragma unroll
      for (int r = 0; r < 16; ++r) {
        int row = d0 + wm * 64 + i * 32 + (r & 3) + 8 * (r >> 2) + 4 * (lane >> 5);
        int col = f0 + wn * 32 + (lane & 31);
        po[(long)row * FF + col] = acc[i][r];
      }
  }
}

#define SCAN_BLOCKS PP
#define RED_BLOCKS (DM * FF / (256 * 4))

__device__ __forceinline__ float gelu_tanh(float x) {
  const float k0 = 0.7978845608028654f, k1 = 0.044715f;
  float inner = k0 * (x + k1 * x * x * x);
  return 0.5f * x * (1.f + tanhf(inner));
}

__global__ __launch_bounds__(256) void k_scan(const float* __restrict__ Rt, const u16* __restrict__ Lre,
                                               const u16* __restrict__ Lim, const u16* __restrict__ logdt,
                                               u16* __restrict__ hsT, const float* __restrict__ part,
                                               const u16* __restrict__ b1, u16* __restrict__ u) {
  __shared__ float2 wtot[4];
  if (blockIdx.x < SCAN_BLOCKS) {
    const int p = blockIdx.x;
    const int tid = threadIdx.x, lane = tid & 63, wid = tid >> 6;
    const float* pr = Rt + (long)p * SEQ + tid * 8;
    const float* pi = Rt + (long)(PP + p) * SEQ + tid * 8;
    f32x4 vr0 = *(const f32x4*)pr, vr1 = *(const f32x4*)(pr + 4);
    f32x4 vi0 = *(const f32x4*)pi, vi1 = *(const f32x4*)(pi + 4);
    float dt = rbf(expf(bf2f(logdt[p])));
    float lr = bf2f(Lre[p]), li = bf2f(Lim[p]);
    float er = expf(dt * lr);
    float sn, cs;
    sincosf(dt * li, &sn, &cs);
    float ar = er * cs, ai = er * sn;
    float den = lr * lr + li * li;
    float nr = ar - 1.0f, ni = ai;
    float cr = (nr * lr + ni * li) / den, ci = (ni * lr - nr * li) / den;
    float hr[8], hi[8];
#pragma unroll
    for (int i = 0; i < 8; ++i) {
      float x = i < 4 ? vr0[i & 3] : vr1[i & 3];
      float y = i < 4 ? vi0[i & 3] : vi1[i & 3];
      hr[i] = cr * x - ci * y;
      hi[i] = cr * y + ci * x;
    }
#pragma unroll
    for (int i = 1; i < 8; ++i) {
      float xr = ar * hr[i - 1] - ai * hi[i - 1] + hr[i];
      float xi = ar * hi[i - 1] + ai * hr[i - 1] + hi[i];
      hr[i] = xr; hi[i] = xi;
    }
    float Mr = ar, Mi = ai;
#pragma unroll
    for (int s = 0; s < 3; ++s) { float tr = Mr * Mr - Mi * Mi, ti = 2.f * Mr * Mi; Mr = tr; Mi = ti; }
    float vr = hr[7], vi = hi[7];
    float Lr = 1.f, Li = 0.f;
#pragma unroll
    for (int s = 0; s < 6; ++s) {
      int o = 1 << s;
      float qr = __shfl_up(vr, o), qi = __shfl_up(vi, o);
      if (lane >= o) {
        vr += Mr * qr - Mi * qi;
        vi += Mr * qi + Mi * qr;
      }
      if ((lane >> s) & 1) { float tr = Lr * Mr - Li * Mi, ti = Lr * Mi + Li * Mr; Lr = tr; Li = ti; }
      float tr = Mr * Mr - Mi * Mi, ti = 2.f * Mr * Mi; Mr = tr; Mi = ti;
    }
    if (lane == 63) wtot[wid] = make_float2(vr, vi);
    float er_ = __shfl_up(vr, 1), ei_ = __shfl_up(vi, 1);
    if (lane == 0) { er_ = 0.f; ei_ = 0.f; }
    __syncthreads();
    float c_r = 0.f, c_i = 0.f;
#pragma unroll
    for (int w = 0; w < 3; ++w) {
      if (w < wid) {
        float2 t = wtot[w];
        float tr = Mr * c_r - Mi * c_i + t.x, ti = Mr * c_i + Mi * c_r + t.y;
        c_r = tr; c_i = ti;
      }
    }
    float gr = er_ + Lr * c_r - Li * c_i, gi = ei_ + Lr * c_i + Li * c_r;
    bf16x8 ore, oim;
#pragma unroll
    for (int i = 0; i < 8; ++i) {
      float tr = ar * gr - ai * gi, ti = ar * gi + ai * gr;
      gr = tr; gi = ti;
      ore[i] = (short)f2bf(hr[i] + gr);
      oim[i] = (short)f2bf(-(hi[i] + gi));
    }
    *(bf16x8*)(hsT + (long)p * SEQ + tid * 8) = ore;
    *(bf16x8*)(hsT + (long)(PP + p) * SEQ + tid * 8) = oim;
  } else {
    long idx = ((long)(blockIdx.x - SCAN_BLOCKS) * 256 + threadIdx.x) * 4;
    f32x4 s = *(const f32x4*)(part + idx);
#pragma unroll
    for (int k = 1; k < KSPLIT; ++k) {
      f32x4 v = *(const f32x4*)(part + (long)k * DM * FF + idx);
      s += v;
    }
    int f = (int)(idx % FF);
    bf16x4 bb = *(const bf16x4*)(b1 + f);
    bf16x4 o;
#pragma unroll
    for (int j = 0; j < 4; ++j) {
      float v = rbf(rbf(s[j]) + bf2f((u16)bb[j]));
      o[j] = (short)f2bf(gelu_tanh(v));
    }
    *(bf16x4*)(u + idx) = o;
  }
}

#define K3_NST 4
#define K3_STAGE 32768
#define K3_NT 1024
__global__ __launch_bounds__(K3_NT) void k_gemm3(const u16* __restrict__ hsT, const u16* __restrict__ Cre,
                                                  const u16* __restrict__ Cim, const u16* __restrict__ w2,
                                                  const u16* __restrict__ u, const u16* __restrict__ x,
                                                  const u16* __restrict__ xn, const u16* __restrict__ Dsk,
                                                  const u16* __restrict__ b2, float* __restrict__ out) {
  __shared__ __attribute__((aligned(16))) char smem[K3_NST * K3_STAGE];
  const int tid = threadIdx.x, lane = tid & 63, wid = tid >> 6;
  const int xcd = blockIdx.x & 7, loc = blockIdx.x >> 3;
  const int mt = xcd * 4 + (loc & 3), nt = loc >> 2;
  const int s0 = mt * 64, d0 = nt * 64;
  const int orow = tid >> 4, ocol = (tid & 15) * 4;
  const long o = (long)(s0 + orow) * DM + d0 + ocol;
  const bf16x4 xv = *(const bf16x4*)(x + o);
  const bf16x4 xnv = *(const bf16x4*)(xn + o);
  const bf16x4 dk = *(const bf16x4*)(Dsk + d0 + ocol);
  const float b2v = bf2f(b2[s0 + orow]);

  const int ksl = wid & 7, hf = ksl >> 2, ks = ksl & 3, nh = wid >> 3;
  f32x16 acc1[2], acc2[2];
#pragma unroll
  for (int i = 0; i < 2; ++i)
#pragma unroll
    for (int r = 0; r < 16; ++r) { acc1[i][r] = 0.f; acc2[i][r] = 0.f; }
  constexpr int nk1 = (2 * PP) / 128, nk2 = FF / 128, nk = nk1 + nk2;
  const int slot = tid & 511;
  const bool isA = tid < 512;
  const unsigned voA = (ks64_row(slot) * SEQ + ks64_col(slot)) * 2;
  const unsigned voB1 = (kc_row(slot) * PP + kc_col(slot)) * 2;
  const unsigned voB2 = (kc_row(slot) * FF + kc_col(slot)) * 2;
  auto stage = [&](int k, int buf) {
#pragma unroll
    for (int h2 = 0; h2 < 2; ++h2) {
      unsigned t = lds_addr(smem) + buf * K3_STAGE + h2 * 16384 + (isA ? 0 : 8192);
      const u16* src;
      unsigned vo;
      if (k < nk1) {
        int k64 = k * 2 + h2;
        const u16* cb = (k < nk1 / 2) ? Cre + k64 * 64 : Cim + (k64 - nk1) * 64;
        src = isA ? hsT + (long)(k64 * 64) * SEQ + s0 : cb + (long)d0 * PP;
        vo = isA ? voA : voB1;
      } else {
        int k64 = (k - nk1) * 2 + h2;
        src = isA ? w2 + (long)(k64 * 64) * SEQ + s0 : u + (long)d0 * FF + k64 * 64;
        vo = isA ? voA : voB2;
      }
      put(src, vo, t, slot);
    }
  };
  auto compute = [&](int k, int buf) {
    const char* a = smem + buf * K3_STAGE + hf * 16384;
    const char* b = a + 8192;
    bf16x8 fa[2], fb;
#pragma unroll
    for (int i = 0; i < 2; ++i) fa[i] = frag_ks64(a, i, ks, lane);
    fb = frag_kc(b, nh, ks, lane);
    if (k < nk1) {
#pragma unroll
      for (int i = 0; i < 2; ++i) acc1[i] = MFMA32(fa[i], fb, acc1[i]);
    } else {
#pragma unroll
      for (int i = 0; i < 2; ++i) acc2[i] = MFMA32(fa[i], fb, acc2[i]);
    }
  };
  pipeline<K3_NST, 2>(nk, stage, compute);

  float* red = (float*)smem;
  f32x4 s1, s2;
#pragma unroll
  for (int pass = 0; pass < 2; ++pass) {
    __syncthreads();
#pragma unroll
    for (int i = 0; i < 2; ++i)
#pragma unroll
      for (int r = 0; r < 16; ++r) {
        int row = i * 32 + (r & 3) + 8 * (r >> 2) + 4 * (lane >> 5);
        int col = nh * 32 + (lane & 31);
        red[ksl * 4096 + row * 64 + col] = pass == 0 ? acc1[i][r] : acc2[i][r];
      }
    __syncthreads();
    f32x4 t = *(const f32x4*)(red + orow * 64 + ocol);
#pragma unroll
    for (int w = 1; w < 8; ++w) t += *(const f32x4*)(red + w * 4096 + orow * 64 + ocol);
    if (pass == 0) s1 = t; else s2 = t;
  }
  f32x4 res;
#pragma unroll
  for (int e = 0; e < 4; ++e) {
    float s = 2.0f * s1[e] + rbf(bf2f((u16)xnv[e]) * bf2f((u16)dk[e]));
    float y2 = rbf(rbf(s2[e]) + b2v);
    float g = rbf(y2 / (1.0f + expf(-y2)));
    res[e] = bf2f((u16)xv[e]) + s * g;
  }
  *(f32x4*)(out + o) = res;
}

extern "C" void kernel_launch(void* const* d_in, const int* in_sizes, int n_in, void* d_out, int out_size,
                              void* d_ws, size_t ws_size, hipStream_t stream) {
  const u16* x = (const u16*)d_in[0];
  const u16* Lre = (const u16*)d_in[1];
  const u16* Lim = (const u16*)d_in[2];
  const u16* Bre = (const u16*)d_in[3];
  const u16* Bim = (const u16*)d_in[4];
  const u16* Cre = (const u16*)d_in[5];
  const u16* Cim = (const u16*)d_in[6];
  const u16* logdt = (const u16*)d_in[7];
  const u16* Dsk = (const u16*)d_in[8];
  const u16* w1 = (const u16*)d_in[10];
  const u16* b1 = (const u16*)d_in[11];
  const u16* w2 = (const u16*)d_in[12];
  const u16* b2 = (const u16*)d_in[13];
  float* out = (float*)d_out;

  char* ws = (char*)d_ws;
  u16* xn = (u16*)ws;                       ws += (size_t)SEQ * DM * 2;
  u16* h = (u16*)ws;                        ws += (size_t)SEQ * DM * 2;
  float* Rt = (float*)ws;                   ws += (size_t)SEQ * 2 * PP * 4;
  float* part = (float*)ws;                 ws += (size_t)KSPLIT * DM * FF * 4;
  u16* u = (u16*)ws;                        ws += (size_t)DM * FF * 2;
  u16* hsT = (u16*)ws;                      ws += (size_t)SEQ * 2 * PP * 2;

  k_ln<<<SEQ / 4, 256, 0, stream>>>(x, xn, h);
  k_gemm1<<<K1_BLOCKS, K1_NT, 0, stream>>>(xn, h, Bre, Bim, w1, Rt, part);
  k_scan<<<SCAN_BLOCKS + RED_BLOCKS, 256, 0, stream>>>(Rt, Lre, Lim, logdt, hsT, part, b1, u);
  k_gemm3<<<256, K3_NT, 0, stream>>>(hsT, Cre, Cim, w2, u, x, xn, Dsk, b2, out);
}
```

```cpp
#include <hip/hip_runtime.h>
#include <hip/hip_bf16.h>

#define SEQ 2048
#define DM 512
#define PP 512
#define FF 1792

typedef unsigned short u16;
typedef __attribute__((ext_vector_type(8))) short bf16x8;
typedef __attribute__((ext_vector_type(4))) short bf16x4;
typedef __attribute__((ext_vector_type(16))) float f32x16;
typedef __attribute__((ext_vector_type(4))) float f32x4;
typedef __attribute__((address_space(3))) const char* lds_cptr;
typedef __attribute__((address_space(3))) bf16x4 lds_bf16x4;

__device__ __forceinline__ float bf2f(u16 v) { return __uint_as_float(((unsigned)v) << 16); }
__device__ __forceinline__ u16 f2bf(float f) {
  unsigned u = __float_as_uint(f);
  u += 0x7fffu + ((u >> 16) & 1u);
  return (u16)(u >> 16);
}
__device__ __forceinline__ float rbf(float f) { return bf2f(f2bf(f)); }

__device__ __forceinline__ float wave_sum(float v) {
#pragma unroll
  for (int o = 32; o > 0; o >>= 1) v += __shfl_xor(v, o);
  return v;
}

#define SPAN_BEGIN(id)
#define SPAN_END(id)
__device__ __forceinline__ void ln_bf16(const float (&in)[8], float (&out)[8]) {
  float s = 0.f;
#pragma unroll
  for (int j = 0; j < 8; ++j) s += in[j];
  s = wave_sum(s);
  float mean = s * (1.0f / DM);
  float q = 0.f;
#pragma unroll
  for (int j = 0; j < 8; ++j) { float d = in[j] - mean; q += d * d; }
  q = wave_sum(q);
  float var = rbf(q * (1.0f / DM));
  float m = rbf(mean);
  float sd = rbf(sqrtf(rbf(var + 1e-5f)));
#pragma unroll
  for (int j = 0; j < 8; ++j) out[j] = rbf(rbf(in[j] - m) / sd);
}

__global__ __launch_bounds__(256) void k_ln(const u16* __restrict__ x, u16* __restrict__ xn, u16* __restrict__ h) {
  SPAN_BEGIN(0)
  int row = blockIdx.x * 4 + (threadIdx.x >> 6);
  int lane = threadIdx.x & 63;
  const u16* xr = x + (long)row * DM + lane * 8;
  bf16x8 v = *(const bf16x8*)xr;
  float in[8], o1[8], o2[8];
#pragma unroll
  for (int j = 0; j < 8; ++j) in[j] = bf2f((u16)v[j]);
  ln_bf16(in, o1);
  ln_bf16(o1, o2);
  bf16x8 w1, w2;
#pragma unroll
  for (int j = 0; j < 8; ++j) { w1[j] = (short)f2bf(o1[j]); w2[j] = (short)f2bf(o2[j]); }
  *(bf16x8*)(xn + (long)row * DM + lane * 8) = w1;
  *(bf16x8*)(h + (long)row * DM + lane * 8) = w2;
  SPAN_END(0)
}

__device__ __forceinline__ void glds16(const void* sbase, unsigned voff, unsigned lds_wave_base) {
  unsigned keep;
  asm volatile("s_mov_b32 %0, m0\n\ts_mov_b32 m0, %3\n\ts_nop 0\n\tglobal_load_lds_dwordx4 %1, %2\n\ts_mov_b32 m0, %0"
               : "=&s"(keep) : "v"(voff), "s"(sbase), "s"(lds_wave_base) : "memory");
}
__device__ __forceinline__ unsigned lds_addr(const void* p) { return (unsigned)(size_t)p; }
__device__ __forceinline__ unsigned uni(unsigned v) { return __builtin_amdgcn_readfirstlane(v); }
__device__ __forceinline__ const void* uni_ptr(const void* p) {
  unsigned long long v = (unsigned long long)p;
  unsigned lo = __builtin_amdgcn_readfirstlane((unsigned)v), hi = __builtin_amdgcn_readfirstlane((unsigned)(v >> 32));
  return (const void*)(((unsigned long long)hi << 32) | lo);
}

__device__ __forceinline__ int kc_row(int slot) { return slot >> 3; }
__device__ __forceinline__ int kc_col(int slot) { int row = slot >> 3; return ((slot & 7) ^ ((row >> 1) & 7)) * 8; }
__device__ __forceinline__ int ks128_row(int slot) { return slot >> 4; }
__device__ __forceinline__ int ks128_col(int slot) { int row = slot >> 4; return ((slot & 15) ^ (((row & 3) << 2) | ((row >> 2) & 3))) * 8; }
__device__ __forceinline__ int ks64_row(int slot) { return (slot >> 6) * 8 + ((slot >> 2) & 7); }
__device__ __forceinline__ int ks64_col(int slot) { int row = ks64_row(slot); return (4 * ((slot >> 5) & 1) + ((slot & 3) ^ ((row >> 2) & 3))) * 8; }
__device__ __forceinline__ void put(const void* sbase, unsigned voff, unsigned tile, int slot) { glds16(uni_ptr(sbase), voff, uni(tile + (slot & ~63) * 16)); }

__device__ __forceinline__ bf16x8 frag_kc(const char* lds, int blk, int ks, int lane) {
  int row = blk * 32 + (lane & 31);
  int ch = ks * 2 + (lane >> 5);
  return *(const bf16x8*)(lds + row * 128 + ((ch ^ ((row >> 1) & 7)) << 4));
}
__device__ __forceinline__ bf16x8 frag_ks128(const char* lds, int blk, int ks, int lane) {
  int hh = lane >> 5, b2 = (lane >> 4) & 1, q = (lane & 15) >> 2, p = lane & 3;
  bf16x8 r;
#pragma unroll
  for (int t = 0; t < 2; ++t) {
    int row = 16 * ks + 8 * hh + 4 * t + q;
    int ch = 4 * blk + 2 * b2 + (p >> 1);
    int off = 256 * row + 16 * (ch ^ (((row & 3) << 2) | ((row >> 2) & 3))) + 8 * (p & 1);
    bf16x4 v = __builtin_amdgcn_ds_read_tr16_b64_v4i16((lds_bf16x4*)(lds_cptr)(lds + off));
    r[4 * t + 0] = v[0]; r[4 * t + 1] = v[1]; r[4 * t + 2] = v[2]; r[4 * t + 3] = v[3];
  }
  return r;
}
__device__ __forceinline__ bf16x8 frag_ks64(const char* lds, int blk, int ks, int lane) {
  int hh = lane >> 5, b2 = (lane >> 4) & 1, q = (lane & 15) >> 2, p = lane & 3;
  bf16x8 r;
#pragma unroll
  for (int t = 0; t < 2; ++t) {
    int row = 16 * ks + 8 * hh + 4 * t + q;
    int ch = 4 * blk + 2 * b2 + (p >> 1);
    int off = 1024 * (row >> 3) + 512 * (ch >> 2) + 64 * (row & 7) + 16 * ((ch & 3) ^ ((row >> 2) & 3)) + 8 * (p & 1);
    bf16x4 v = __builtin_amdgcn_ds_read_tr16_b64_v4i16((lds_bf16x4*)(lds_cptr)(lds + off));
    r[4 * t + 0] = v[0]; r[4 * t + 1] = v[1]; r[4 * t + 2] = v[2]; r[4 * t + 3] = v[3];
  }
  return r;
}

#define ST_WT(p, v) __hip_atomic_store((p), (v), __ATOMIC_RELAXED, __HIP_MEMORY_SCOPE_AGENT)
#define MFMA32(a, b, c) __builtin_amdgcn_mfma_f32_32x32x16_bf16(a, b, c, 0, 0, 0)

template <int NST, int LPS, class StageF, class ComputeF>
__device__ __forceinline__ void pipeline(int nk, StageF stage, ComputeF compute) {
#pragma unroll
  for (int s = 0; s < NST - 1; ++s)
    if (s < nk) stage(s, s);
  for (int k = 0; k < nk; ++k) {
    if (k + NST - 2 < nk) {
      asm volatile("s_waitcnt vmcnt(%0)" ::"n"((NST - 2) * LPS) : "memory");
    } else {
      asm volatile("s_waitcnt vmcnt(0)" ::: "memory");
    }
    __builtin_amdgcn_s_barrier();
    if (k + NST - 1 < nk) stage(k + NST - 1, (k + NST - 1) % NST);
    compute(k, k % NST);
  }
}

#define KSPLIT 4
#define K1_BLOCKS (128 + 4 * 14 * KSPLIT)
#define K1_NST 2
#define K1_NT 512

__global__ __launch_bounds__(K1_NT, 4) void k_gemm1(const u16* __restrict__ xn, const u16* __restrict__ h,
                                                   const u16* __restrict__ Bre, const u16* __restrict__ Bim,
                                                   const u16* __restrict__ w1, float* __restrict__ Rt,
                                                   float* __restrict__ part) {
  __shared__ __attribute__((aligned(16))) char smem[K1_NST * 32768];
  SPAN_BEGIN(1)
  const int tid = threadIdx.x, lane = tid & 63, wid = tid >> 6, wm = wid >> 2, wn = wid & 3;
  f32x16 acc[2];
#pragma unroll
  for (int i = 0; i < 2; ++i)
#pragma unroll
    for (int r = 0; r < 16; ++r) acc[i][r] = 0.f;

  const int xcd = blockIdx.x & 7, loc = blockIdx.x >> 3;
  if (loc >= 28) {
    const int l = loc - 28;
    const int mt = l & 7, nt = xcd * 2 + (l >> 3);
    const int m0 = mt * 128, t0 = nt * 128;
    const u16* bbase = (mt < 4) ? Bre + (long)m0 * DM : Bim + (long)(m0 - PP) * DM;
    auto stage = [&](int k, int buf) {
      unsigned a = lds_addr(smem) + buf * 32768;
      unsigned b = a + 16384;
#pragma unroll
      for (int i = 0; i < 2; ++i) {
        int slot = i * K1_NT + tid;
        unsigned vo = (kc_row(slot) * DM + kc_col(slot)) * 2;
        put(bbase + k * 64, vo, a, slot);
        put(xn + (long)t0 * DM + k * 64, vo, b, slot);
      }
    };
    auto compute = [&](int k, int buf) {
      const char* a = smem + buf * 32768;
      const char* b = a + 16384;
#pragma unroll
      for (int ks = 0; ks < 4; ++ks) {
        bf16x8 fa[2], fb;
#pragma unroll
        for (int i = 0; i < 2; ++i) fa[i] = frag_kc(a, wm * 2 + i, ks, lane);
        fb = frag_kc(b, wn, ks, lane);
#pragma unroll
        for (int i = 0; i < 2; ++i) acc[i] = MFMA32(fa[i], fb, acc[i]);
      }
    };
    pipeline<K1_NST, 4>(DM / 64, stage, compute);
#pragma unroll
    for (int i = 0; i < 2; ++i)
#pragma unroll
      for (int r = 0; r < 16; ++r) {
        int row = m0 + wm * 64 + i * 32 + (r & 3) + 8 * (r >> 2) + 4 * (lane >> 5);
        int col = t0 + wn * 32 + (lane & 31);
        ST_WT(&Rt[(long)row * SEQ + col], acc[i][r]);
      }
  } else {
    const int ksp = xcd >> 1;
    const int mt = (xcd & 1) * 2 + (loc & 1), nt = loc >> 1;
    const int d0 = mt * 128, f0 = nt * 128;
    const int kbeg = ksp * (SEQ / KSPLIT);
    auto stage = [&](int k, int buf) {
      unsigned a = lds_addr(smem) + buf * 32768;
      unsigned bb = a + 16384;
#pragma unroll
      for (int i = 0; i < 2; ++i) {
        int slot = i * K1_NT + tid;
        put(h + (long)(kbeg + k * 64) * DM + d0, (ks128_row(slot) * DM + ks128_col(slot)) * 2, a, slot);
        put(w1 + (long)(kbeg + k * 64) * FF + f0, (ks128_row(slot) * FF + ks128_col(slot)) * 2, bb, slot);
      }
    };
    auto compute = [&](int k, int buf) {
      const char* a = smem + buf * 32768;
      const char* bb = a + 16384;
#pragma unroll
      for (int ks = 0; ks < 4; ++ks) {
        bf16x8 fa[2], fb;
#pragma unroll
        for (int i = 0; i < 2; ++i) fa[i] = frag_ks128(a, wm * 2 + i, ks, lane);
        fb = frag_ks128(bb, wn, ks, lane);
#pragma unroll
        for (int i = 0; i < 2; ++i) acc[i] = MFMA32(fa[i], fb, acc[i]);
      }
    };
    pipeline<K1_NST, 4>(SEQ / KSPLIT / 64, stage, compute);
    float* po = part + (long)ksp * DM * FF;
#pragma unroll
    for (int i = 0; i < 2; ++i)
#pragma unroll
      for (int r = 0; r < 16; ++r) {
        int row = d0 + wm * 64 + i * 32 + (r & 3) + 8 * (r >> 2) + 4 * (lane >> 5);
        int col = f0 + wn * 32 + (lane & 31);
        ST_WT(&po[(long)row * FF + col], acc[i][r]);
      }
  }
  SPAN_END(1)
}

#define SCAN_BLOCKS PP
#define RED_BLOCKS (DM * FF / (256 * 4))

__device__ __forceinline__ float gelu_tanh(float x) {
  const float k0 = 0.7978845608028654f, k1 = 0.044715f;
  float inner = k0 * (x + k1 * x * x * x);
  return 0.5f * x * (1.f + tanhf(inner));
}

__global__ __launch_bounds__(256) void k_scan(const float* __restrict__ Rt, const u16* __restrict__ Lre,
                                               const u16* __restrict__ Lim, const u16* __restrict__ logdt,
                                               u16* __restrict__ hsT, const float* __restrict__ part,
                                               const u16* __restrict__ b1, u16* __restrict__ u,
                                               const u16* __restrict__ w2pf) {
  __shared__ float2 wtot[4];
  SPAN_BEGIN(2)
  if (blockIdx.x < SCAN_BLOCKS) {
    const int p = blockIdx.x;
    const int tid = threadIdx.x, lane = tid & 63, wid = tid >> 6;
    const float* pr = Rt + (long)p * SEQ + tid * 8;
    const float* pi = Rt + (long)(PP + p) * SEQ + tid * 8;
    f32x4 vr0 = *(const f32x4*)pr, vr1 = *(const f32x4*)(pr + 4);
    f32x4 vi0 = *(const f32x4*)pi, vi1 = *(const f32x4*)(pi + 4);
    float dt = rbf(expf(bf2f(logdt[p])));
    float lr = bf2f(Lre[p]), li = bf2f(Lim[p]);
    float er = expf(dt * lr);
    float sn, cs;
    sincosf(dt * li, &sn, &cs);
    float ar = er * cs, ai = er * sn;
    float den = lr * lr + li * li;
    float nr = ar - 1.0f, ni = ai;
    float cr = (nr * lr + ni * li) / den, ci = (ni * lr - nr * li) / den;
    float hr[8], hi[8];
#pragma unroll
    for (int i = 0; i < 8; ++i) {
      float x = i < 4 ? vr0[i & 3] : vr1[i & 3];
      float y = i < 4 ? vi0[i & 3] : vi1[i & 3];
      hr[i] = cr * x - ci * y;
      hi[i] = cr * y + ci * x;
    }
#pragma unroll
    for (int i = 1; i < 8; ++i) {
      float xr = ar * hr[i - 1] - ai * hi[i - 1] + hr[i];
      float xi = ar * hi[i - 1] + ai * hr[i - 1] + hi[i];
      hr[i] = xr; hi[i] = xi;
    }
    float Mr = ar, Mi = ai;
#pragma unroll
    for (int s = 0; s < 3; ++s) { float tr = Mr * Mr - Mi * Mi, ti = 2.f * Mr * Mi; Mr = tr; Mi = ti; }
    float vr = hr[7], vi = hi[7];
    float Lr = 1.f, Li = 0.f;
#pragma unroll
    for (int s = 0; s < 6; ++s) {
      int o = 1 << s;
      float qr = __shfl_up(vr, o), qi = __shfl_up(vi, o);
      if (lane >= o) {
        vr += Mr * qr - Mi * qi;
        vi += Mr * qi + Mi * qr;
      }
      if ((lane >> s) & 1) { float tr = Lr * Mr - Li * Mi, ti = Lr * Mi + Li * Mr; Lr = tr; Li = ti; }
      float tr = Mr * Mr - Mi * Mi, ti = 2.f * Mr * Mi; Mr = tr; Mi = ti;
    }
    if (lane == 63) wtot[wid] = make_float2(vr, vi);
    float er_ = __shfl_up(vr, 1), ei_ = __shfl_up(vi, 1);
    if (lane == 0) { er_ = 0.f; ei_ = 0.f; }
    __syncthreads();
    float c_r = 0.f, c_i = 0.f;
#pragma unroll
    for (int w = 0; w < 3; ++w) {
      if (w < wid) {
        float2 t = wtot[w];
        float tr = Mr * c_r - Mi * c_i + t.x, ti = Mr * c_i + Mi * c_r + t.y;
        c_r = tr; c_i = ti;
      }
    }
    float gr = er_ + Lr * c_r - Li * c_i, gi = ei_ + Lr * c_i + Li * c_r;
    bf16x8 ore, oim;
#pragma unroll
    for (int i = 0; i < 8; ++i) {
      float tr = ar * gr - ai * gi, ti = ar * gi + ai * gr;
      gr = tr; gi = ti;
      ore[i] = (short)f2bf(hr[i] + gr);
      oim[i] = (short)f2bf(-(hi[i] + gi));
    }
    *(bf16x8*)(hsT + (long)p * SEQ + tid * 8) = ore;
    *(bf16x8*)(hsT + (long)(PP + p) * SEQ + tid * 8) = oim;
  } else {
    long idx = ((long)(blockIdx.x - SCAN_BLOCKS) * 256 + threadIdx.x) * 4;
    f32x4 s = *(const f32x4*)(part + idx);
#pragma unroll
    for (int k = 1; k < KSPLIT; ++k) {
      f32x4 v = *(const f32x4*)(part + (long)k * DM * FF + idx);
      s += v;
    }
    int f = (int)(idx % FF);
    bf16x4 bb = *(const bf16x4*)(b1 + f);
    bf16x4 o;
#pragma unroll
    for (int j = 0; j < 4; ++j) {
      float v = rbf(rbf(s[j]) + bf2f((u16)bb[j]));
      o[j] = (short)f2bf(gelu_tanh(v));
    }
    *(bf16x4*)(u + idx) = o;
  }
  SPAN_END(2)
}

#define K3_NST 4
#define K3_PD 6
#define K3_STAGE 32768
#define K3_NT 1024
__global__ __launch_bounds__(K3_NT) void k_gemm3(const u16* __restrict__ hsT, const u16* __restrict__ Cre,
                                                  const u16* __restrict__ Cim, const u16* __restrict__ w2,
                                                  const u16* __restrict__ u, const u16* __restrict__ x,
                                                  const u16* __restrict__ xn, const u16* __restrict__ Dsk,
                                                  const u16* __restrict__ b2, float* __restrict__ out) {
  __shared__ __attribute__((aligned(16))) char smem[K3_NST * K3_STAGE];
  const int tid = threadIdx.x, lane = tid & 63, wid = tid >> 6;
  const int xcd = blockIdx.x & 7, loc = blockIdx.x >> 3;
  const int mt = xcd * 4 + (loc & 3), nt = loc >> 2;
  const int s0 = mt * 64, d0 = nt * 64;
  const int orow = tid >> 4, ocol = (tid & 15) * 4;
  const long o = (long)(s0 + orow) * DM + d0 + ocol;
  const bf16x4 xv = *(const bf16x4*)(x + o);
  const bf16x4 xnv = *(const bf16x4*)(xn + o);
  const bf16x4 dk = *(const bf16x4*)(Dsk + d0 + ocol);
  const float b2v = bf2f(b2[s0 + orow]);

  const int kg = wid & 3, mh = (wid >> 2) & 1, nh = wid >> 3;
  f32x16 acc1, acc2;
#pragma unroll
  for (int r = 0; r < 16; ++r) { acc1[r] = 0.f; acc2[r] = 0.f; }
  constexpr int nk1 = (2 * PP) / 128, nk2 = FF / 128, nk = nk1 + nk2;
  const int slot = tid & 511;
  const bool isA = tid < 512;
  const unsigned voA = (ks64_row(slot) * SEQ + ks64_col(slot)) * 2;
  const unsigned voB1 = (kc_row(slot) * PP + kc_col(slot)) * 2;
  const unsigned voB2 = (kc_row(slot) * FF + kc_col(slot)) * 2;
  auto stage = [&](int k, int buf) {
#pragma unroll
    for (int h2 = 0; h2 < 2; ++h2) {
      unsigned t = lds_addr(smem) + buf * K3_STAGE + h2 * 16384 + (isA ? 0 : 8192);
      const u16* src;
      unsigned vo;
      if (k < nk1) {
        int k64 = k * 2 + h2;
        const u16* cb = (k < nk1 / 2) ? Cre + k64 * 64 : Cim + (k64 - nk1) * 64;
        src = isA ? hsT + (long)(k64 * 64) * SEQ + s0 : cb + (long)d0 * PP;
        vo = isA ? voA : voB1;
      } else {
        int k64 = (k - nk1) * 2 + h2;
        src = isA ? w2 + (long)(k64 * 64) * SEQ + s0 : u + (long)d0 * FF + k64 * 64;
        vo = isA ? voA : voB2;
      }
      put(src, vo, t, slot);
    }
  };
  auto compute = [&](int k, int buf) {
    bf16x8 fa[2], fb[2];
#pragma unroll
    for (int hf = 0; hf < 2; ++hf) {
      const char* a = smem + buf * K3_STAGE + hf * 16384;
      const char* b = a + 8192;
      fa[hf] = frag_ks64(a, mh, kg, lane);
      fb[hf] = frag_kc(b, nh, kg, lane);
    }
    if (k < nk1) {
#pragma unroll
      for (int hf = 0; hf < 2; ++hf) acc1 = MFMA32(fa[hf], fb[hf], acc1);
    } else {
#pragma unroll
      for (int hf = 0; hf < 2; ++hf) acc2 = MFMA32(fa[hf], fb[hf], acc2);
    }
  };
  pipeline<K3_NST, 2>(nk, stage, compute);

  float* red = (float*)smem;
  __syncthreads();
#pragma unroll
  for (int r = 0; r < 16; ++r) {
    int row = mh * 32 + (r & 3) + 8 * (r >> 2) + 4 * (lane >> 5);
    int col = nh * 32 + (lane & 31);
    red[kg * 4096 + row * 64 + col] = acc1[r];
    red[(4 + kg) * 4096 + row * 64 + col] = acc2[r];
  }
  __syncthreads();
  f32x4 s1 = *(const f32x4*)(red + orow * 64 + ocol);
  f32x4 s2 = *(const f32x4*)(red + 4 * 4096 + orow * 64 + ocol);
#pragma unroll
  for (int w = 1; w < 4; ++w) {
    s1 += *(const f32x4*)(red + w * 4096 + orow * 64 + ocol);
    s2 += *(const f32x4*)(red + (4 + w) * 4096 + orow * 64 + ocol);
  }
  f32x4 res;
#pragma unroll
  for (int e = 0; e < 4; ++e) {
    float s = 2.0f * s1[e] + rbf(bf2f((u16)xnv[e]) * bf2f((u16)dk[e]));
    float y2 = rbf(rbf(s2[e]) + b2v);
    float g = rbf(y2 / (1.0f + expf(-y2)));
    res[e] = bf2f((u16)xv[e]) + s * g;
  }
  *(f32x4*)(out + o) = res;
}

#define K3B_NST 3
#define K3B_STAGE 49152
__global__ __launch_bounds__(1024) void k_gemm3b(const u16* __restrict__ hsT, const u16* __restrict__ Cre,
                                                 const u16* __restrict__ Cim, const u16* __restrict__ w2,
                                                 const u16* __restrict__ u, const u16* __restrict__ x,
                                                 const u16* __restrict__ xn, const u16* __restrict__ Dsk,
                                                 const u16* __restrict__ b2, float* __restrict__ out) {
  __shared__ __attribute__((aligned(16))) char smem[K3B_NST * K3B_STAGE];
  const int tid = threadIdx.x, lane = tid & 63, wid = tid >> 6;
  const int xcd = blockIdx.x & 7, loc = blockIdx.x >> 3;
  const int mt = xcd * 2 + (loc & 1), nt = loc >> 1;
  const int s0 = mt * 128, d0 = nt * 64;
  const int orow = tid >> 3, ocol = (tid & 7) * 8;
  const long o = (long)(s0 + orow) * DM + d0 + ocol;
  const bf16x8 xv = *(const bf16x8*)(x + o);
  const bf16x8 xnv = *(const bf16x8*)(xn + o);
  const bf16x8 dk = *(const bf16x8*)(Dsk + d0 + ocol);
  const float b2v = bf2f(b2[s0 + orow]);

  const int kg = wid & 1, mb = (wid >> 1) & 3, nb = wid >> 3;
  f32x16 acc1, acc2;
#pragma unroll
  for (int r = 0; r < 16; ++r) { acc1[r] = 0.f; acc2[r] = 0.f; }
  constexpr int nk1 = (2 * PP) / 128, nk2 = FF / 128, nk = nk1 + nk2;
  const int bslot = tid & 511, bh = tid >> 9;
  const unsigned voA = (ks128_row(tid) * SEQ + ks128_col(tid)) * 2;
  const unsigned voB1 = (kc_row(bslot) * PP + kc_col(bslot)) * 2;
  const unsigned voB2 = (kc_row(bslot) * FF + kc_col(bslot)) * 2;
  auto stage = [&](int k, int buf) {
    unsigned base = lds_addr(smem) + buf * K3B_STAGE;
    if (k < nk1) {
#pragma unroll
      for (int h2 = 0; h2 < 2; ++h2) put(hsT + (long)((k * 2 + h2) * 64) * SEQ + s0, voA, base + h2 * 16384, tid);
      int k64 = k * 2 + bh;
      const u16* cb = (k < nk1 / 2) ? Cre + k64 * 64 : Cim + (k64 - nk1) * 64;
      put(cb + (long)d0 * PP, voB1, base + 32768 + bh * 8192, bslot);
    } else {
      int kk = k - nk1;
#pragma unroll
      for (int h2 = 0; h2 < 2; ++h2) put(w2 + (long)((kk * 2 + h2) * 64) * SEQ + s0, voA, base + h2 * 16384, tid);
      put(u + (long)d0 * FF + (kk * 2 + bh) * 64, voB2, base + 32768 + bh * 8192, bslot);
    }
  };
  auto compute = [&](int k, int buf) {
    bf16x8 fa[4], fb[4];
#pragma unroll
    for (int j = 0; j < 4; ++j) {
      int q = kg + 2 * j, hf = q >> 2, ks = q & 3;
      fa[j] = frag_ks128(smem + buf * K3B_STAGE + hf * 16384, mb, ks, lane);
      fb[j] = frag_kc(smem + buf * K3B_STAGE + 32768 + hf * 8192, nb, ks, lane);
    }
    if (k < nk1) {
#pragma unroll
      for (int j = 0; j < 4; ++j) acc1 = MFMA32(fa[j], fb[j], acc1);
    } else {
#pragma unroll
      for (int j = 0; j < 4; ++j) acc2 = MFMA32(fa[j], fb[j], acc2);
    }
  };
  pipeline<K3B_NST, 3>(nk, stage, compute);
  float* red = (float*)smem;
  __syncthreads();
#pragma unroll
  for (int r = 0; r < 16; ++r) {
    int row = mb * 32 + (r & 3) + 8 * (r >> 2) + 4 * (lane >> 5);
    int col = nb * 32 + (lane & 31);
    red[kg * 8192 + row * 64 + col] = acc1[r];
    red[(2 + kg) * 8192 + row * 64 + col] = acc2[r];
  }
  __syncthreads();
#pragma unroll
  for (int c4 = 0; c4 < 2; ++c4) {
    f32x4 s1 = *(const f32x4*)(red + orow * 64 + ocol + c4 * 4) + *(const f32x4*)(red + 8192 + orow * 64 + ocol + c4 * 4);
    f32x4 s2 = *(const f32x4*)(red + 2 * 8192 + orow * 64 + ocol + c4 * 4) + *(const f32x4*)(red + 3 * 8192 + orow * 64 + ocol + c4 * 4);
    f32x4 res;
#pragma unroll
    for (int e = 0; e < 4; ++e) {
      float s = 2.0f * s1[e] + rbf(bf2f((u16)xnv[c4 * 4 + e]) * bf2f((u16)dk[c4 * 4 + e]));
      float y2 = rbf(rbf(s2[e]) + b2v);
      float g = rbf(y2 / (1.0f + expf(-y2)));
      res[e] = bf2f((u16)xv[c4 * 4 + e]) + s * g;
    }
    *(f32x4*)(out + o + c4 * 4) = res;
  }
}

extern "C" void kernel_launch(void* const* d_in, const int* in_sizes, int n_in, void* d_out, int out_size,
                              void* d_ws, size_t ws_size, hipStream_t stream) {
  const u16* x = (const u16*)d_in[0];
  const u16* Lre = (const u16*)d_in[1];
  const u16* Lim = (const u16*)d_in[2];
  const u16* Bre = (const u16*)d_in[3];
  const u16* Bim = (const u16*)d_in[4];
  const u16* Cre = (const u16*)d_in[5];
  const u16* Cim = (const u16*)d_in[6];
  const u16* logdt = (const u16*)d_in[7];
  const u16* Dsk = (const u16*)d_in[8];
  const u16* w1 = (const u16*)d_in[10];
  const u16* b1 = (const u16*)d_in[11];
  const u16* w2 = (const u16*)d_in[12];
  const u16* b2 = (const u16*)d_in[13];
  float* out = (float*)d_out;

  char* ws = (char*)d_ws;
  u16* xn = (u16*)ws;                       ws += (size_t)SEQ * DM * 2;
  u16* h = (u16*)ws;                        ws += (size_t)SEQ * DM * 2;
  float* Rt = (float*)ws;                   ws += (size_t)SEQ * 2 * PP * 4;
  float* part = (float*)ws;                 ws += (size_t)KSPLIT * DM * FF * 4;
  u16* u = (u16*)ws;                        ws += (size_t)DM * FF * 2;
  u16* hsT = (u16*)ws;                      ws += (size_t)SEQ * 2 * PP * 2;

  k_ln<<<SEQ / 4, 256, 0, stream>>>(x, xn, h);
  k_gemm1<<<K1_BLOCKS, K1_NT, 0, stream>>>(xn, h, Bre, Bim, w1, Rt, part);
  k_scan<<<SCAN_BLOCKS + RED_BLOCKS, 256, 0, stream>>>(Rt, Lre, Lim, logdt, hsT, part, b1, u, w2);
  k_gemm3<<<256, K3_NT, 0, stream>>>(hsT, Cre, Cim, w2, u, x, xn, Dsk, b2, out);
}
```

```cpp
#define W2PF 1
#include <hip/hip_runtime.h>
#include <hip/hip_bf16.h>

#define SEQ 2048
#define DM 512
#define PP 512
#define FF 1792

typedef unsigned short u16;
typedef __attribute__((ext_vector_type(8))) short bf16x8;
typedef __attribute__((ext_vector_type(4))) short bf16x4;
typedef __attribute__((ext_vector_type(16))) float f32x16;
typedef __attribute__((ext_vector_type(4))) float f32x4;
typedef __attribute__((address_space(3))) const char* lds_cptr;
typedef __attribute__((address_space(3))) bf16x4 lds_bf16x4;

__device__ __forceinline__ float bf2f(u16 v) { return __uint_as_float(((unsigned)v) << 16); }
__device__ __forceinline__ u16 f2bf(float f) {
  unsigned u = __float_as_uint(f);
  u += 0x7fffu + ((u >> 16) & 1u);
  return (u16)(u >> 16);
}
__device__ __forceinline__ float rbf(float f) { return bf2f(f2bf(f)); }

__device__ __forceinline__ float wave_sum(float v) {
#pragma unroll
  for (int o = 32; o > 0; o >>= 1) v += __shfl_xor(v, o);
  return v;
}

#define SPAN_BEGIN(id)
#define SPAN_END(id)
__device__ __forceinline__ void ln_bf16(const float (&in)[8], float (&out)[8]) {
  float s = 0.f;
#pragma unroll
  for (int j = 0; j < 8; ++j) s += in[j];
  s = wave_sum(s);
  float mean = s * (1.0f / DM);
  float q = 0.f;
#pragma unroll
  for (int j = 0; j < 8; ++j) { float d = in[j] - mean; q += d * d; }
  q = wave_sum(q);
  float var = rbf(q * (1.0f / DM));
  float m = rbf(mean);
  float sd = rbf(sqrtf(rbf(var + 1e-5f)));
#pragma unroll
  for (int j = 0; j < 8; ++j) out[j] = rbf(rbf(in[j] - m) / sd);
}

__global__ __launch_bounds__(256) void k_ln(const u16* __restrict__ x, u16* __restrict__ xn, u16* __restrict__ h,
                                            const u16* __restrict__ w1pf) {
  SPAN_BEGIN(0)
  int row = blockIdx.x * 4 + (threadIdx.x >> 6);
  int lane = threadIdx.x & 63;
  const u16* xr = x + (long)row * DM + lane * 8;
  bf16x8 v = *(const bf16x8*)xr;
  float in[8], o1[8], o2[8];
#pragma unroll
  for (int j = 0; j < 8; ++j) in[j] = bf2f((u16)v[j]);
  ln_bf16(in, o1);
  ln_bf16(o1, o2);
  bf16x8 w1, w2;
#pragma unroll
  for (int j = 0; j < 8; ++j) { w1[j] = (short)f2bf(o1[j]); w2[j] = (short)f2bf(o2[j]); }
  *(bf16x8*)(xn + (long)row * DM + lane * 8) = w1;
  *(bf16x8*)(h + (long)row * DM + lane * 8) = w2;
  SPAN_END(0)
}

__device__ __forceinline__ void glds16(const void* sbase, unsigned voff, unsigned lds_wave_base) {
  unsigned keep;
  asm volatile("s_mov_b32 %0, m0\n\ts_mov_b32 m0, %3\n\ts_nop 0\n\tglobal_load_lds_dwordx4 %1, %2\n\ts_mov_b32 m0, %0"
               : "=&s"(keep) : "v"(voff), "s"(sbase), "s"(lds_wave_base) : "memory");
}
__device__ __forceinline__ unsigned lds_addr(const void* p) { return (unsigned)(size_t)p; }
__device__ __forceinline__ unsigned uni(unsigned v) { return __builtin_amdgcn_readfirstlane(v); }
__device__ __forceinline__ const void* uni_ptr(const void* p) {
  unsigned long long v = (unsigned long long)p;
  unsigned lo = __builtin_amdgcn_readfirstlane((unsigned)v), hi = __builtin_amdgcn_readfirstlane((unsigned)(v >> 32));
  return (const void*)(((unsigned long long)hi << 32) | lo);
}

__device__ __forceinline__ int kc_row(int slot) { return slot >> 3; }
__device__ __forceinline__ int kc_col(int slot) { int row = slot >> 3; return ((slot & 7) ^ ((row >> 1) & 7)) * 8; }
__device__ __forceinline__ int ks128_row(int slot) { return slot >> 4; }
__device__ __forceinline__ int ks128_col(int slot) { int row = slot >> 4; return ((slot & 15) ^ (((row & 3) << 2) | ((row >> 2) & 3))) * 8; }
__device__ __forceinline__ int ks64_row(int slot) { return (slot >> 6) * 8 + ((slot >> 2) & 7); }
__device__ __forceinline__ int ks64_col(int slot) { int row = ks64_row(slot); return (4 * ((slot >> 5) & 1) + ((slot & 3) ^ ((row >> 2) & 3))) * 8; }
__device__ __forceinline__ void put(const void* sbase, unsigned voff, unsigned tile, int slot) { glds16(uni_ptr(sbase), voff, uni(tile + (slot & ~63) * 16)); }

__device__ __forceinline__ bf16x8 frag_kc(const char* lds, int blk, int ks, int lane) {
  int row = blk * 32 + (lane & 31);
  int ch = ks * 2 + (lane >> 5);
  return *(const bf16x8*)(lds + row * 128 + ((ch ^ ((row >> 1) & 7)) << 4));
}
__device__ __forceinline__ bf16x8 frag_ks128(const char* lds, int blk, int ks, int lane) {
  int hh = lane >> 5, b2 = (lane >> 4) & 1, q = (lane & 15) >> 2, p = lane & 3;
  bf16x8 r;
#pragma unroll
  for (int t = 0; t < 2; ++t) {
    int row = 16 * ks + 8 * hh + 4 * t + q;
    int ch = 4 * blk + 2 * b2 + (p >> 1);
    int off = 256 * row + 16 * (ch ^ (((row & 3) << 2) | ((row >> 2) & 3))) + 8 * (p & 1);
    bf16x4 v = __builtin_amdgcn_ds_read_tr16_b64_v4i16((lds_bf16x4*)(lds_cptr)(lds + off));
    r[4 * t + 0] = v[0]; r[4 * t + 1] = v[1]; r[4 * t + 2] = v[2]; r[4 * t + 3] = v[3];
  }
  return r;
}
__device__ __forceinline__ bf16x8 frag_ks64(const char* lds, int blk, int ks, int lane) {
  int hh = lane >> 5, b2 = (lane >> 4) & 1, q = (lane & 15) >> 2, p = lane & 3;
  bf16x8 r;
#pragma unroll
  for (int t = 0; t < 2; ++t) {
    int row = 16 * ks + 8 * hh + 4 * t + q;
    int ch = 4 * blk + 2 * b2 + (p >> 1);
    int off = 1024 * (row >> 3) + 512 * (ch >> 2) + 64 * (row & 7) + 16 * ((ch & 3) ^ ((row >> 2) & 3)) + 8 * (p & 1);
    bf16x4 v = __builtin_amdgcn_ds_read_tr16_b64_v4i16((lds_bf16x4*)(lds_cptr)(lds + off));
    r[4 * t + 0] = v[0]; r[4 * t + 1] = v[1]; r[4 * t + 2] = v[2]; r[4 * t + 3] = v[3];
  }
  return r;
}

#define ST_WT(p, v) __hip_atomic_store((p), (v), __ATOMIC_RELAXED, __HIP_MEMORY_SCOPE_AGENT)
#define MFMA32(a, b, c) __builtin_amdgcn_mfma_f32_32x32x16_bf16(a, b, c, 0, 0, 0)

template <int NST, int LPS, class StageF, class ComputeF>
__device__ __forceinline__ void pipeline(int nk, StageF stage, ComputeF compute) {
#pragma unroll
  for (int s = 0; s < NST - 1; ++s)
    if (s < nk) stage(s, s);
  for (int k = 0; k < nk; ++k) {
    if (k + NST - 2 < nk) {
      asm volatile("s_waitcnt vmcnt(%0)" ::"n"((NST - 2) * LPS) : "memory");
    } else {
      asm volatile("s_waitcnt vmcnt(0)" ::: "memory");
    }
    __builtin_amdgcn_s_barrier();
    if (k + NST - 1 < nk) stage(k + NST - 1, (k + NST - 1) % NST);
    compute(k, k % NST);
  }
}

#define KSPLIT 4
#define K1_BLOCKS (128 + 4 * 14 * KSPLIT)
#define K1_NST 2
#define K1_NT 512

__global__ __launch_bounds__(K1_NT, 4) void k_gemm1(const u16* __restrict__ xn, const u16* __restrict__ h,
                                                   const u16* __restrict__ Bre, const u16* __restrict__ Bim,
                                                   const u16* __restrict__ w1, float* __restrict__ Rt,
                                                   float* __restrict__ part) {
  __shared__ __attribute__((aligned(16))) char smem[K1_NST * 32768];
  SPAN_BEGIN(1)
  const int tid = threadIdx.x, lane = tid & 63, wid = tid >> 6, wm = wid >> 2, wn = wid & 3;
  f32x16 acc[2];
#pragma unroll
  for (int i = 0; i < 2; ++i)
#pragma unroll
    for (int r = 0; r < 16; ++r) acc[i][r] = 0.f;

  const int xcd = blockIdx.x & 7, loc = blockIdx.x >> 3;
  if (loc >= 28) {
    const int l = loc - 28;
    const int mt = l & 7, nt = xcd * 2 + (l >> 3);
    const int m0 = mt * 128, t0 = nt * 128;
    const u16* bbase = (mt < 4) ? Bre + (long)m0 * DM : Bim + (long)(m0 - PP) * DM;
    auto stage = [&](int k, int buf) {
      unsigned a = lds_addr(smem) + buf * 32768;
      unsigned b = a + 16384;
#pragma unroll
      for (int i = 0; i < 2; ++i) {
        int slot = i * K1_NT + tid;
        unsigned vo = (kc_row(slot) * DM + kc_col(slot)) * 2;
        put(bbase + k * 64, vo, a, slot);
        put(xn + (long)t0 * DM + k * 64, vo, b, slot);
      }
    };
    auto compute = [&](int k, int buf) {
      const char* a = smem + buf * 32768;
      const char* b = a + 16384;
#pragma unroll
      for (int ks = 0; ks < 4; ++ks) {
        bf16x8 fa[2], fb;
#pragma unroll
        for (int i = 0; i < 2; ++i) fa[i] = frag_kc(a, wm * 2 + i, ks, lane);
        fb = frag_kc(b, wn, ks, lane);
#pragma unroll
        for (int i = 0; i < 2; ++i) acc[i] = MFMA32(fa[i], fb, acc[i]);
      }
    };
    pipeline<K1_NST, 4>(DM / 64, stage, compute);
#pragma unroll
    for (int i = 0; i < 2; ++i)
#pragma unroll
      for (int r = 0; r < 16; ++r) {
        int row = m0 + wm * 64 + i * 32 + (r & 3) + 8 * (r >> 2) + 4 * (lane >> 5);
        int col = t0 + wn * 32 + (lane & 31);
        ST_WT(&Rt[(long)row * SEQ + col], acc[i][r]);
      }
  } else {
    const int ksp = xcd >> 1;
    const int mt = (xcd & 1) * 2 + (loc & 1), nt = loc >> 1;
    const int d0 = mt * 128, f0 = nt * 128;
    const int kbeg = ksp * (SEQ / KSPLIT);
    auto stage = [&](int k, int buf) {
      unsigned a = lds_addr(smem) + buf * 32768;
      unsigned bb = a + 16384;
#pragma unroll
      for (int i = 0; i < 2; ++i) {
        int slot = i * K1_NT + tid;
        put(h + (long)(kbeg + k * 64) * DM + d0, (ks128_row(slot) * DM + ks128_col(slot)) * 2, a, slot);
        put(w1 + (long)(kbeg + k * 64) * FF + f0, (ks128_row(slot) * FF + ks128_col(slot)) * 2, bb, slot);
      }
    };
    auto compute = [&](int k, int buf) {
      const char* a = smem + buf * 32768;
      const char* bb = a + 16384;
#pragma unroll
      for (int ks = 0; ks < 4; ++ks) {
        bf16x8 fa[2], fb;
#pragma unroll
        for (int i = 0; i < 2; ++i) fa[i] = frag_ks128(a, wm * 2 + i, ks, lane);
        fb = frag_ks128(bb, wn, ks, lane);
#pragma unroll
        for (int i = 0; i < 2; ++i) acc[i] = MFMA32(fa[i], fb, acc[i]);
      }
    };
    pipeline<K1_NST, 4>(SEQ / KSPLIT / 64, stage, compute);
    float* po = part + (long)ksp * DM * FF;
#pragma unroll
    for (int i = 0; i < 2; ++i)
#pragma unroll
      for (int r = 0; r < 16; ++r) {
        int row = d0 + wm * 64 + i * 32 + (r & 3) + 8 * (r >> 2) + 4 * (lane >> 5);
        int col = f0 + wn * 32 + (lane & 31);
        ST_WT(&po[(long)row * FF + col], acc[i][r]);
      }
  }
  SPAN_END(1)
}

#define SCAN_BLOCKS PP
#define RED_BLOCKS (DM * FF / (256 * 4))

__device__ __forceinline__ float gelu_tanh(float x) {
  const float k0 = 0.7978845608028654f, k1 = 0.044715f;
  float inner = k0 * (x + k1 * x * x * x);
  return 0.5f * x * (1.f + tanhf(inner));
}

__global__ __launch_bounds__(256) void k_scan(const float* __restrict__ Rt, const u16* __restrict__ Lre,
                                               const u16* __restrict__ Lim, const u16* __restrict__ logdt,
                                               u16* __restrict__ hsT, const float* __restrict__ part,
                                               const u16* __restrict__ b1, u16* __restrict__ u,
                                               const u16* __restrict__ w2pf) {
  __shared__ float2 wtot[4];
  if (blockIdx.x >= SCAN_BLOCKS + RED_BLOCKS) {
    const int b = blockIdx.x - (SCAN_BLOCKS + RED_BLOCKS);
    const uint4* p = (const uint4*)w2pf + (long)b * 256 * 8 + threadIdx.x;
    uint4 acc4 = p[0];
#pragma unroll
    for (int i = 1; i < 8; ++i) { uint4 v = p[i * 256]; acc4.x ^= v.x; acc4.y ^= v.y; acc4.z ^= v.z; acc4.w ^= v.w; }
    if ((acc4.x ^ acc4.y ^ acc4.z ^ acc4.w) == 0x12345678u) wtot[0].x = 1.f;
    return;
  }
  SPAN_BEGIN(2)
  if (blockIdx.x < SCAN_BLOCKS) {
    const int p = blockIdx.x;
    const int tid = threadIdx.x, lane = tid & 63, wid = tid >> 6;
    const float* pr = Rt + (long)p * SEQ + tid * 8;
    const float* pi = Rt + (long)(PP + p) * SEQ + tid * 8;
    f32x4 vr0 = *(const f32x4*)pr, vr1 = *(const f32x4*)(pr + 4);
    f32x4 vi0 = *(const f32x4*)pi, vi1 = *(const f32x4*)(pi + 4);
    float dt = rbf(expf(bf2f(logdt[p])));
    float lr = bf2f(Lre[p]), li = bf2f(Lim[p]);
    float er = expf(dt * lr);
    float sn, cs;
    sincosf(dt * li, &sn, &cs);
    float ar = er * cs, ai = er * sn;
    float den = lr * lr + li * li;
    float nr = ar - 1.0f, ni = ai;
    float cr = (nr * lr + ni * li) / den, ci = (ni * lr - nr * li) / den;
    float hr[8], hi[8];
#pragma unroll
    for (int i = 0; i < 8; ++i) {
      float x = i < 4 ? vr0[i & 3] : vr1[i & 3];
      float y = i < 4 ? vi0[i & 3] : vi1[i & 3];
      hr[i] = cr * x - ci * y;
      hi[i] = cr * y + ci * x;
    }
#pragma unroll
    for (int i = 1; i < 8; ++i) {
      float xr = ar * hr[i - 1] - ai * hi[i - 1] + hr[i];
      float xi = ar * hi[i - 1] + ai * hr[i - 1] + hi[i];
      hr[i] = xr; hi[i] = xi;
    }
    float Mr = ar, Mi = ai;
#pragma unroll
    for (int s = 0; s < 3; ++s) { float tr = Mr * Mr - Mi * Mi, ti = 2.f * Mr * Mi; Mr = tr; Mi = ti; }
    float vr = hr[7], vi = hi[7];
    float Lr = 1.f, Li = 0.f;
#pragma unroll
    for (int s = 0; s < 6; ++s) {
      int o = 1 << s;
      float qr = __shfl_up(vr, o), qi = __shfl_up(vi, o);
      if (lane >= o) {
        vr += Mr * qr - Mi * qi;
        vi += Mr * qi + Mi * qr;
      }
      if ((lane >> s) & 1) { float tr = Lr * Mr - Li * Mi, ti = Lr * Mi + Li * Mr; Lr = tr; Li = ti; }
      float tr = Mr * Mr - Mi * Mi, ti = 2.f * Mr * Mi; Mr = tr; Mi = ti;
    }
    if (lane == 63) wtot[wid] = make_float2(vr, vi);
    float er_ = __shfl_up(vr, 1), ei_ = __shfl_up(vi, 1);
    if (lane == 0) { er_ = 0.f; ei_ = 0.f; }
    __syncthreads();
    float c_r = 0.f, c_i = 0.f;
#pragma unroll
    for (int w = 0; w < 3; ++w) {
      if (w < wid) {
        float2 t = wtot[w];
        float tr = Mr * c_r - Mi * c_i + t.x, ti = Mr * c_i + Mi * c_r + t.y;
        c_r = tr; c_i = ti;
      }
    }
    float gr = er_ + Lr * c_r - Li * c_i, gi = ei_ + Lr * c_i + Li * c_r;
    bf16x8 ore, oim;
#pragma unroll
    for (int i = 0; i < 8; ++i) {
      float tr = ar * gr - ai * gi, ti = ar * gi + ai * gr;
      gr = tr; gi = ti;
      ore[i] = (short)f2bf(hr[i] + gr);
      oim[i] = (short)f2bf(-(hi[i] + gi));
    }
    *(bf16x8*)(hsT + (long)p * SEQ + tid * 8) = ore;
    *(bf16x8*)(hsT + (long)(PP + p) * SEQ + tid * 8) = oim;
  } else {
    long idx = ((long)(blockIdx.x - SCAN_BLOCKS) * 256 + threadIdx.x) * 4;
    f32x4 s = *(const f32x4*)(part + idx);
#pragma unroll
    for (int k = 1; k < KSPLIT; ++k) {
      f32x4 v = *(const f32x4*)(part + (long)k * DM * FF + idx);
      s += v;
    }
    int f = (int)(idx % FF);
    bf16x4 bb = *(const bf16x4*)(b1 + f);
    bf16x4 o;
#pragma unroll
    for (int j = 0; j < 4; ++j) {
      float v = rbf(rbf(s[j]) + bf2f((u16)bb[j]));
      o[j] = (short)f2bf(gelu_tanh(v));
    }
    *(bf16x4*)(u + idx) = o;
  }
  SPAN_END(2)
}

#define K3_NST 4
#define K3_PD 6
#define K3_STAGE 32768
#define K3_NT 1024
__global__ __launch_bounds__(K3_NT) void k_gemm3(const u16* __restrict__ hsT, const u16* __restrict__ Cre,
                                                  const u16* __restrict__ Cim, const u16* __restrict__ w2,
                                                  const u16* __restrict__ u, const u16* __restrict__ x,
                                                  const u16* __restrict__ xn, const u16* __restrict__ Dsk,
                                                  const u16* __restrict__ b2, float* __restrict__ out) {
  __shared__ __attribute__((aligned(16))) char smem[K3_NST * K3_STAGE];
  const int tid = threadIdx.x, lane = tid & 63, wid = tid >> 6;
  const int xcd = blockIdx.x & 7, loc = blockIdx.x >> 3;
  const int mt = xcd * 4 + (loc & 3), nt = loc >> 2;
  const int s0 = mt * 64, d0 = nt * 64;
  const int orow = tid >> 4, ocol = (tid & 15) * 4;
  const long o = (long)(s0 + orow) * DM + d0 + ocol;
  const bf16x4 xv = *(const bf16x4*)(x + o);
  const bf16x4 xnv = *(const bf16x4*)(xn + o);
  const bf16x4 dk = *(const bf16x4*)(Dsk + d0 + ocol);
  const float b2v = bf2f(b2[s0 + orow]);

  const int kg = wid & 3, mh = (wid >> 2) & 1, nh = wid >> 3;
  f32x16 acc1, acc2;
#pragma unroll
  for (int r = 0; r < 16; ++r) { acc1[r] = 0.f; acc2[r] = 0.f; }
  constexpr int nk1 = (2 * PP) / 128, nk2 = FF / 128, nk = nk1 + nk2;
  const int slot = tid & 511;
  const bool isA = tid < 512;
  const unsigned voA = (ks64_row(slot) * SEQ + ks64_col(slot)) * 2;
  const unsigned voB1 = (kc_row(slot) * PP + kc_col(slot)) * 2;
  const unsigned voB2 = (kc_row(slot) * FF + kc_col(slot)) * 2;
  auto stage = [&](int k, int buf) {
#pragma unroll
    for (int h2 = 0; h2 < 2; ++h2) {
      unsigned t = lds_addr(smem) + buf * K3_STAGE + h2 * 16384 + (isA ? 0 : 8192);
      const u16* src;
      unsigned vo;
      if (k < nk1) {
        int k64 = k * 2 + h2;
        const u16* cb = (k < nk1 / 2) ? Cre + k64 * 64 : Cim + (k64 - nk1) * 64;
        src = isA ? hsT + (long)(k64 * 64) * SEQ + s0 : cb + (long)d0 * PP;
        vo = isA ? voA : voB1;
      } else {
        int k64 = (k - nk1) * 2 + h2;
        src = isA ? w2 + (long)(k64 * 64) * SEQ + s0 : u + (long)d0 * FF + k64 * 64;
        vo = isA ? voA : voB2;
      }
      put(src, vo, t, slot);
    }
  };
  auto compute = [&](int k, int buf) {
    bf16x8 fa[2], fb[2];
#pragma unroll
    for (int hf = 0; hf < 2; ++hf) {
      const char* a = smem + buf * K3_STAGE + hf * 16384;
      const char* b = a + 8192;
      fa[hf] = frag_ks64(a, mh, kg, lane);
      fb[hf] = frag_kc(b, nh, kg, lane);
    }
    if (k < nk1) {
#pragma unroll
      for (int hf = 0; hf < 2; ++hf) acc1 = MFMA32(fa[hf], fb[hf], acc1);
    } else {
#pragma unroll
      for (int hf = 0; hf < 2; ++hf) acc2 = MFMA32(fa[hf], fb[hf], acc2);
    }
  };
  pipeline<K3_NST, 2>(nk, stage, compute);

  float* red = (float*)smem;
  __syncthreads();
#pragma unroll
  for (int r = 0; r < 16; ++r) {
    int row = mh * 32 + (r & 3) + 8 * (r >> 2) + 4 * (lane >> 5);
    int col = nh * 32 + (lane & 31);
    red[kg * 4096 + row * 64 + col] = acc1[r];
    red[(4 + kg) * 4096 + row * 64 + col] = acc2[r];
  }
  __syncthreads();
  f32x4 s1 = *(const f32x4*)(red + orow * 64 + ocol);
  f32x4 s2 = *(const f32x4*)(red + 4 * 4096 + orow * 64 + ocol);
#pragma unroll
  for (int w = 1; w < 4; ++w) {
    s1 += *(const f32x4*)(red + w * 4096 + orow * 64 + ocol);
    s2 += *(const f32x4*)(red + (4 + w) * 4096 + orow * 64 + ocol);
  }
  f32x4 res;
#pragma unroll
  for (int e = 0; e < 4; ++e) {
    float s = 2.0f * s1[e] + rbf(bf2f((u16)xnv[e]) * bf2f((u16)dk[e]));
    float y2 = rbf(rbf(s2[e]) + b2v);
    float g = rbf(y2 / (1.0f + expf(-y2)));
    res[e] = bf2f((u16)xv[e]) + s * g;
  }
  *(f32x4*)(out + o) = res;
}

#define K3B_NST 3
#define K3B_STAGE 49152
__global__ __launch_bounds__(1024) void k_gemm3b(const u16* __restrict__ hsT, const u16* __restrict__ Cre,
                                                 const u16* __restrict__ Cim, const u16* __restrict__ w2,
                                                 const u16* __restrict__ u, const u16* __restrict__ x,
                                                 const u16* __restrict__ xn, const u16* __restrict__ Dsk,
                                                 const u16* __restrict__ b2, float* __restrict__ out) {
  __shared__ __attribute__((aligned(16))) char smem[K3B_NST * K3B_STAGE];
  const int tid = threadIdx.x, lane = tid & 63, wid = tid >> 6;
  const int xcd = blockIdx.x & 7, loc = blockIdx.x >> 3;
  const int mt = xcd * 2 + (loc & 1), nt = loc >> 1;
  const int s0 = mt * 128, d0 = nt * 64;
  const int orow = tid >> 3, ocol = (tid & 7) * 8;
  const long o = (long)(s0 + orow) * DM + d0 + ocol;
  const bf16x8 xv = *(const bf16x8*)(x + o);
  const bf16x8 xnv = *(const bf16x8*)(xn + o);
  const bf16x8 dk = *(const bf16x8*)(Dsk + d0 + ocol);
  const float b2v = bf2f(b2[s0 + orow]);

  const int kg = wid & 1, mb = (wid >> 1) & 3, nb = wid >> 3;
  f32x16 acc1, acc2;
#pragma unroll
  for (int r = 0; r < 16; ++r) { acc1[r] = 0.f; acc2[r] = 0.f; }
  constexpr int nk1 = (2 * PP) / 128, nk2 = FF / 128, nk = nk1 + nk2;
  const int bslot = tid & 511, bh = tid >> 9;
  const unsigned voA = (ks128_row(tid) * SEQ + ks128_col(tid)) * 2;
  const unsigned voB1 = (kc_row(bslot) * PP + kc_col(bslot)) * 2;
  const unsigned voB2 = (kc_row(bslot) * FF + kc_col(bslot)) * 2;
  auto stage = [&](int k, int buf) {
    unsigned base = lds_addr(smem) + buf * K3B_STAGE;
    if (k < nk1) {
#pragma unroll
      for (int h2 = 0; h2 < 2; ++h2) put(hsT + (long)((k * 2 + h2) * 64) * SEQ + s0, voA, base + h2 * 16384, tid);
      int k64 = k * 2 + bh;
      const u16* cb = (k < nk1 / 2) ? Cre + k64 * 64 : Cim + (k64 - nk1) * 64;
      put(cb + (long)d0 * PP, voB1, base + 32768 + bh * 8192, bslot);
    } else {
      int kk = k - nk1;
#pragma unroll
      for (int h2 = 0; h2 < 2; ++h2) put(w2 + (long)((kk * 2 + h2) * 64) * SEQ + s0, voA, base + h2 * 16384, tid);
      put(u + (long)d0 * FF + (kk * 2 + bh) * 64, voB2, base + 32768 + bh * 8192, bslot);
    }
  };
  auto compute = [&](int k, int buf) {
    bf16x8 fa[4], fb[4];
#pragma unroll
    for (int j = 0; j < 4; ++j) {
      int q = kg + 2 * j, hf = q >> 2, ks = q & 3;
      fa[j] = frag_ks128(smem + buf * K3B_STAGE + hf * 16384, mb, ks, lane);
      fb[j] = frag_kc(smem + buf * K3B_STAGE + 32768 + hf * 8192, nb, ks, lane);
    }
    if (k < nk1) {
#pragma unroll
      for (int j = 0; j < 4; ++j) acc1 = MFMA32(fa[j], fb[j], acc1);
    } else {
#pragma unroll
      for (int j = 0; j < 4; ++j) acc2 = MFMA32(fa[j], fb[j], acc2);
    }
  };
  pipeline<K3B_NST, 3>(nk, stage, compute);
  float* red = (float*)smem;
  __syncthreads();
#pragma unroll
  for (int r = 0; r < 16; ++r) {
    int row = mb * 32 + (r & 3) + 8 * (r >> 2) + 4 * (lane >> 5);
    int col = nb * 32 + (lane & 31);
    red[kg * 8192 + row * 64 + col] = acc1[r];
    red[(2 + kg) * 8192 + row * 64 + col] = acc2[r];
  }
  __syncthreads();
#pragma unroll
  for (int c4 = 0; c4 < 2; ++c4) {
    f32x4 s1 = *(const f32x4*)(red + orow * 64 + ocol + c4 * 4) + *(const f32x4*)(red + 8192 + orow * 64 + ocol + c4 * 4);
    f32x4 s2 = *(const f32x4*)(red + 2 * 8192 + orow * 64 + ocol + c4 * 4) + *(const f32x4*)(red + 3 * 8192 + orow * 64 + ocol + c4 * 4);
    f32x4 res;
#pragma unroll
    for (int e = 0; e < 4; ++e) {
      float s = 2.0f * s1[e] + rbf(bf2f((u16)xnv[c4 * 4 + e]) * bf2f((u16)dk[c4 * 4 + e]));
      float y2 = rbf(rbf(s2[e]) + b2v);
      float g = rbf(y2 / (1.0f + expf(-y2)));
      res[e] = bf2f((u16)xv[c4 * 4 + e]) + s * g;
    }
    *(f32x4*)(out + o + c4 * 4) = res;
  }
}

extern "C" void kernel_launch(void* const* d_in, const int* in_sizes, int n_in, void* d_out, int out_size,
                              void* d_ws, size_t ws_size, hipStream_t stream) {
  const u16* x = (const u16*)d_in[0];
  const u16* Lre = (const u16*)d_in[1];
  const u16* Lim = (const u16*)d_in[2];
  const u16* Bre = (const u16*)d_in[3];
  const u16* Bim = (const u16*)d_in[4];
  const u16* Cre = (const u16*)d_in[5];
  const u16* Cim = (const u16*)d_in[6];
  const u16* logdt = (const u16*)d_in[7];
  const u16* Dsk = (const u16*)d_in[8];
  const u16* w1 = (const u16*)d_in[10];
  const u16* b1 = (const u16*)d_in[11];
  const u16* w2 = (const u16*)d_in[12];
  const u16* b2 = (const u16*)d_in[13];
  float* out = (float*)d_out;

  char* ws = (char*)d_ws;
  u16* xn = (u16*)ws;                       ws += (size_t)SEQ * DM * 2;
  u16* h = (u16*)ws;                        ws += (size_t)SEQ * DM * 2;
  float* Rt = (float*)ws;                   ws += (size_t)SEQ * 2 * PP * 4;
  float* part = (float*)ws;                 ws += (size_t)KSPLIT * DM * FF * 4;
  u16* u = (u16*)ws;                        ws += (size_t)DM * FF * 2;
  u16* hsT = (u16*)ws;                      ws += (size_t)SEQ * 2 * PP * 2;

  k_ln<<<SEQ / 4, 256, 0, stream>>>(x, xn, h, w1);
  k_gemm1<<<K1_BLOCKS, K1_NT, 0, stream>>>(xn, h, Bre, Bim, w1, Rt, part);
  k_scan<<<SCAN_BLOCKS + RED_BLOCKS + (FF * SEQ * 2) / (256 * 8 * 16), 256, 0, stream>>>(Rt, Lre, Lim, logdt, hsT, part, b1, u, w2);
  k_gemm3<<<256, K3_NT, 0, stream>>>(hsT, Cre, Cim, w2, u, x, xn, Dsk, b2, out);
}
```

```cpp
#define W2PF 1
#define W1PF 1
#include <hip/hip_runtime.h>
#include <hip/hip_bf16.h>

#define SEQ 2048
#define DM 512
#define PP 512
#define FF 1792

typedef unsigned short u16;
typedef __attribute__((ext_vector_type(8))) short bf16x8;
typedef __attribute__((ext_vector_type(4))) short bf16x4;
typedef __attribute__((ext_vector_type(16))) float f32x16;
typedef __attribute__((ext_vector_type(4))) float f32x4;
typedef __attribute__((address_space(3))) const char* lds_cptr;
typedef __attribute__((address_space(3))) bf16x4 lds_bf16x4;

__device__ __forceinline__ float bf2f(u16 v) { return __uint_as_float(((unsigned)v) << 16); }
__device__ __forceinline__ u16 f2bf(float f) {
  unsigned u = __float_as_uint(f);
  u += 0x7fffu + ((u >> 16) & 1u);
  return (u16)(u >> 16);
}
__device__ __forceinline__ float rbf(float f) { return bf2f(f2bf(f)); }

__device__ __forceinline__ float wave_sum(float v) {
#pragma unroll
  for (int o = 32; o > 0; o >>= 1) v += __shfl_xor(v, o);
  return v;
}

#define SPAN_BEGIN(id)
#define SPAN_END(id)
__device__ __forceinline__ void ln_bf16(const float (&in)[8], float (&out)[8]) {
  float s = 0.f;
#pragma unroll
  for (int j = 0; j < 8; ++j) s += in[j];
  s = wave_sum(s);
  float mean = s * (1.0f / DM);
  float q = 0.f;
#pragma unroll
  for (int j = 0; j < 8; ++j) { float d = in[j] - mean; q += d * d; }
  q = wave_sum(q);
  float var = rbf(q * (1.0f / DM));
  float m = rbf(mean);
  float sd = rbf(sqrtf(rbf(var + 1e-5f)));
#pragma unroll
  for (int j = 0; j < 8; ++j) out[j] = rbf(rbf(in[j] - m) / sd);
}

__global__ __launch_bounds__(256) void k_ln(const u16* __restrict__ x, u16* __restrict__ xn, u16* __restrict__ h,
                                            const u16* __restrict__ w1pf) {
  if (blockIdx.x >= SEQ / 4) {
    const int b = blockIdx.x - SEQ / 4;
    const uint4* p = (const uint4*)w1pf + (long)b * 256 * 8 + threadIdx.x;
    uint4 acc4 = p[0];
#pragma unroll
    for (int i = 1; i < 8; ++i) { uint4 v = p[i * 256]; acc4.x ^= v.x; acc4.y ^= v.y; acc4.z ^= v.z; acc4.w ^= v.w; }
    if ((acc4.x ^ acc4.y ^ acc4.z ^ acc4.w) == 0x12345678u) xn[0] = 0;
    return;
  }
  SPAN_BEGIN(0)
  int row = blockIdx.x * 4 + (threadIdx.x >> 6);
  int lane = threadIdx.x & 63;
  const u16* xr = x + (long)row * DM + lane * 8;
  bf16x8 v = *(const bf16x8*)xr;
  float in[8], o1[8], o2[8];
#pragma unroll
  for (int j = 0; j < 8; ++j) in[j] = bf2f((u16)v[j]);
  ln_bf16(in, o1);
  ln_bf16(o1, o2);
  bf16x8 w1, w2;
#pragma unroll
  for (int j = 0; j < 8; ++j) { w1[j] = (short)f2bf(o1[j]); w2[j] = (short)f2bf(o2[j]); }
  *(bf16x8*)(xn + (long)row * DM + lane * 8) = w1;
  *(bf16x8*)(h + (long)row * DM + lane * 8) = w2;
  SPAN_END(0)
}

__device__ __forceinline__ void glds16(const void* sbase, unsigned voff, unsigned lds_wave_base) {
  unsigned keep;
  asm volatile("s_mov_b32 %0, m0\n\ts_mov_b32 m0, %3\n\ts_nop 0\n\tglobal_load_lds_dwordx4 %1, %2\n\ts_mov_b32 m0, %0"
               : "=&s"(keep) : "v"(voff), "s"(sbase), "s"(lds_wave_base) : "memory");
}
__device__ __forceinline__ unsigned lds_addr(const void* p) { return (unsigned)(size_t)p; }
__device__ __forceinline__ unsigned uni(unsigned v) { return __builtin_amdgcn_readfirstlane(v); }
__device__ __forceinline__ const void* uni_ptr(const void* p) {
  unsigned long long v = (unsigned long long)p;
  unsigned lo = __builtin_amdgcn_readfirstlane((unsigned)v), hi = __builtin_amdgcn_readfirstlane((unsigned)(v >> 32));
  return (const void*)(((unsigned long long)hi << 32) | lo);
}

__device__ __forceinline__ int kc_row(int slot) { return slot >> 3; }
__device__ __forceinline__ int kc_col(int slot) { int row = slot >> 3; return ((slot & 7) ^ ((row >> 1) & 7)) * 8; }
__device__ __forceinline__ int ks128_row(int slot) { return slot >> 4; }
__device__ __forceinline__ int ks128_col(int slot) { int row = slot >> 4; return ((slot & 15) ^ (((row & 3) << 2) | ((row >> 2) & 3))) * 8; }
__device__ __forceinline__ int ks64_row(int slot) { return (slot >> 6) * 8 + ((slot >> 2) & 7); }
__device__ __forceinline__ int ks64_col(int slot) { int row = ks64_row(slot); return (4 * ((slot >> 5) & 1) + ((slot & 3) ^ ((row >> 2) & 3))) * 8; }
__device__ __forceinline__ void put(const void* sbase, unsigned voff, unsigned tile, int slot) { glds16(uni_ptr(sbase), voff, uni(tile + (slot & ~63) * 16)); }

__device__ __forceinline__ bf16x8 frag_kc(const char* lds, int blk, int ks, int lane) {
  int row = blk * 32 + (lane & 31);
  int ch = ks * 2 + (lane >> 5);
  return *(const bf16x8*)(lds + row * 128 + ((ch ^ ((row >> 1) & 7)) << 4));
}
__device__ __forceinline__ bf16x8 frag_ks128(const char* lds, int blk, int ks, int lane) {
  int hh = lane >> 5, b2 = (lane >> 4) & 1, q = (lane & 15) >> 2, p = lane & 3;
  bf16x8 r;
#pragma unroll
  for (int t = 0; t < 2; ++t) {
    int row = 16 * ks + 8 * hh + 4 * t + q;
    int ch = 4 * blk + 2 * b2 + (p >> 1);
    int off = 256 * row + 16 * (ch ^ (((row & 3) << 2) | ((row >> 2) & 3))) + 8 * (p & 1);
    bf16x4 v = __builtin_amdgcn_ds_read_tr16_b64_v4i16((lds_bf16x4*)(lds_cptr)(lds + off));
    r[4 * t + 0] = v[0]; r[4 * t + 1] = v[1]; r[4 * t + 2] = v[2]; r[4 * t + 3] = v[3];
  }
  return r;
}
__device__ __forceinline__ bf16x8 frag_ks64(const char* lds, int blk, int ks, int lane) {
  int hh = lane >> 5, b2 = (lane >> 4) & 1, q = (lane & 15) >> 2, p = lane & 3;
  bf16x8 r;
#pragma unroll
  for (int t = 0; t < 2; ++t) {
    int row = 16 * ks + 8 * hh + 4 * t + q;
    int ch = 4 * blk + 2 * b2 + (p >> 1);
    int off = 1024 * (row >> 3) + 512 * (ch >> 2) + 64 * (row & 7) + 16 * ((ch & 3) ^ ((row >> 2) & 3)) + 8 * (p & 1);
    bf16x4 v = __builtin_amdgcn_ds_read_tr16_b64_v4i16((lds_bf16x4*)(lds_cptr)(lds + off));
    r[4 * t + 0] = v[0]; r[4 * t + 1] = v[1]; r[4 * t + 2] = v[2]; r[4 * t + 3] = v[3];
  }
  return r;
}

#define ST_WT(p, v) __hip_atomic_store((p), (v), __ATOMIC_RELAXED, __HIP_MEMORY_SCOPE_AGENT)
#define MFMA32(a, b, c) __builtin_amdgcn_mfma_f32_32x32x16_bf16(a, b, c, 0, 0, 0)

template <int NST, int LPS, class StageF, class ComputeF>
__device__ __forceinline__ void pipeline(int nk, StageF stage, ComputeF compute) {
#pragma unroll
  for (int s = 0; s < NST - 1; ++s)
    if (s < nk) stage(s, s);
  for (int k = 0; k < nk; ++k) {
    if (k + NST - 2 < nk) {
      asm volatile("s_waitcnt vmcnt(%0)" ::"n"((NST - 2) * LPS) : "memory");
    } else {
      asm volatile("s_waitcnt vmcnt(0)" ::: "memory");
    }
    __builtin_amdgcn_s_barrier();
    if (k + NST - 1 < nk) stage(k + NST - 1, (k + NST - 1) % NST);
    compute(k, k % NST);
  }
}

#define KSPLIT 4
#define K1_BLOCKS (128 + 4 * 14 * KSPLIT)
#define K1_NST 2
#define K1_NT 512

__global__ __launch_bounds__(K1_NT, 4) void k_gemm1(const u16* __restrict__ xn, const u16* __restrict__ h,
                                                   const u16* __restrict__ Bre, const u16* __restrict__ Bim,
                                                   const u16* __restrict__ w1, float* __restrict__ Rt,
                                                   float* __restrict__ part) {
  __shared__ __attribute__((aligned(16))) char smem[K1_NST * 32768];
  SPAN_BEGIN(1)
  const int tid = threadIdx.x, lane = tid & 63, wid = tid >> 6, wm = wid >> 2, wn = wid & 3;
  f32x16 acc[2];
#pragma unroll
  for (int i = 0; i < 2; ++i)
#pragma unroll
    for (int r = 0; r < 16; ++r) acc[i][r] = 0.f;

  const int xcd = blockIdx.x & 7, loc = blockIdx.x >> 3;
  if (loc >= 28) {
    const int l = loc - 28;
    const int mt = l & 7, nt = xcd * 2 + (l >> 3);
    const int m0 = mt * 128, t0 = nt * 128;
    const u16* bbase = (mt < 4) ? Bre + (long)m0 * DM : Bim + (long)(m0 - PP) * DM;
    auto stage = [&](int k, int buf) {
      unsigned a = lds_addr(smem) + buf * 32768;
      unsigned b = a + 16384;
#pragma unroll
      for (int i = 0; i < 2; ++i) {
        int slot = i * K1_NT + tid;
        unsigned vo = (kc_row(slot) * DM + kc_col(slot)) * 2;
        put(bbase + k * 64, vo, a, slot);
        put(xn + (long)t0 * DM + k * 64, vo, b, slot);
      }
    };
    auto compute = [&](int k, int buf) {
      const char* a = smem + buf * 32768;
      const char* b = a + 16384;
#pragma unroll
      for (int ks = 0; ks < 4; ++ks) {
        bf16x8 fa[2], fb;
#pragma unroll
        for (int i = 0; i < 2; ++i) fa[i] = frag_kc(a, wm * 2 + i, ks, lane);
        fb = frag_kc(b, wn, ks, lane);
#pragma unroll
        for (int i = 0; i < 2; ++i) acc[i] = MFMA32(fa[i], fb, acc[i]);
      }
    };
    pipeline<K1_NST, 4>(DM / 64, stage, compute);
#pragma unroll
    for (int i = 0; i < 2; ++i)
#pragma unroll
      for (int r = 0; r < 16; ++r) {
        int row = m0 + wm * 64 + i * 32 + (r & 3) + 8 * (r >> 2) + 4 * (lane >> 5);
        int col = t0 + wn * 32 + (lane & 31);
        ST_WT(&Rt[(long)row * SEQ + col], acc[i][r]);
      }
  } else {
    const int ksp = xcd >> 1;
    const int mt = (xcd & 1) * 2 + (loc & 1), nt = loc >> 1;
    const int d0 = mt * 128, f0 = nt * 128;
    const int kbeg = ksp * (SEQ / KSPLIT);
    auto stage = [&](int k, int buf) {
      unsigned a = lds_addr(smem) + buf * 32768;
      unsigned bb = a + 16384;
#pragma unroll
      for (int i = 0; i < 2; ++i) {
        int slot = i * K1_NT + tid;
        put(h + (long)(kbeg + k * 64) * DM + d0, (ks128_row(slot) * DM + ks128_col(slot)) * 2, a, slot);
        put(w1 + (long)(kbeg + k * 64) * FF + f0, (ks128_row(slot) * FF + ks128_col(slot)) * 2, bb, slot);
      }
    };
    auto compute = [&](int k, int buf) {
      const char* a = smem + buf * 32768;
      const char* bb = a + 16384;
#pragma unroll
      for (int ks = 0; ks < 4; ++ks) {
        bf16x8 fa[2], fb;
#pragma unroll
        for (int i = 0; i < 2; ++i) fa[i] = frag_ks128(a, wm * 2 + i, ks, lane);
        fb = frag_ks128(bb, wn, ks, lane);
#pragma unroll
        for (int i = 0; i < 2; ++i) acc[i] = MFMA32(fa[i], fb, acc[i]);
      }
    };
    pipeline<K1_NST, 4>(SEQ / KSPLIT / 64, stage, compute);
    float* po = part + (long)ksp * DM * FF;
#pragma unroll
    for (int i = 0; i < 2; ++i)
#pragma unroll
      for (int r = 0; r < 16; ++r) {
        int row = d0 + wm * 64 + i * 32 + (r & 3) + 8 * (r >> 2) + 4 * (lane >> 5);
        int col = f0 + wn * 32 + (lane & 31);
        ST_WT(&po[(long)row * FF + col], acc[i][r]);
      }
  }
  SPAN_END(1)
}

#define SCAN_BLOCKS PP
#define RED_BLOCKS (DM * FF / (256 * 4))

__device__ __forceinline__ float gelu_tanh(float x) {
  const float k0 = 0.7978845608028654f, k1 = 0.044715f;
  float inner = k0 * (x + k1 * x * x * x);
  return 0.5f * x * (1.f + tanhf(inner));
}

__global__ __launch_bounds__(256) void k_scan(const float* __restrict__ Rt, const u16* __restrict__ Lre,
                                               const u16* __restrict__ Lim, const u16* __restrict__ logdt,
                                               u16* __restrict__ hsT, const float* __restrict__ part,
                                               const u16* __restrict__ b1, u16* __restrict__ u,
                                               const u16* __restrict__ w2pf) {
  __shared__ float2 wtot[4];
  if (blockIdx.x >= SCAN_BLOCKS + RED_BLOCKS) {
    const int b = blockIdx.x - (SCAN_BLOCKS + RED_BLOCKS);
    const uint4* p = (const uint4*)w2pf + (long)b * 256 * 8 + threadIdx.x;
    uint4 acc4 = p[0];
#pragma unroll
    for (int i = 1; i < 8; ++i) { uint4 v = p[i * 256]; acc4.x ^= v.x; acc4.y ^= v.y; acc4.z ^= v.z; acc4.w ^= v.w; }
    if ((acc4.x ^ acc4.y ^ acc4.z ^ acc4.w) == 0x12345678u) wtot[0].x = 1.f;
    return;
  }
  SPAN_BEGIN(2)
  if (blockIdx.x < SCAN_BLOCKS) {
    const int p = blockIdx.x;
    const int tid = threadIdx.x, lane = tid & 63, wid = tid >> 6;
    const float* pr = Rt + (long)p * SEQ + tid * 8;
    const float* pi = Rt + (long)(PP + p) * SEQ + tid * 8;
    f32x4 vr0 = *(const f32x4*)pr, vr1 = *(const f32x4*)(pr + 4);
    f32x4 vi0 = *(const f32x4*)pi, vi1 = *(const f32x4*)(pi + 4);
    float dt = rbf(expf(bf2f(logdt[p])));
    float lr = bf2f(Lre[p]), li = bf2f(Lim[p]);
    float er = expf(dt * lr);
    float sn, cs;
    sincosf(dt * li, &sn, &cs);
    float ar = er * cs, ai = er * sn;
    float den = lr * lr + li * li;
    float nr = ar - 1.0f, ni = ai;
    float cr = (nr * lr + ni * li) / den, ci = (ni * lr - nr * li) / den;
    float hr[8], hi[8];
#pragma unroll
    for (int i = 0; i < 8; ++i) {
      float x = i < 4 ? vr0[i & 3] : vr1[i & 3];
      float y = i < 4 ? vi0[i & 3] : vi1[i & 3];
      hr[i] = cr * x - ci * y;
      hi[i] = cr * y + ci * x;
    }
#pragma unroll
    for (int i = 1; i < 8; ++i) {
      float xr = ar * hr[i - 1] - ai * hi[i - 1] + hr[i];
      float xi = ar * hi[i - 1] + ai * hr[i - 1] + hi[i];
      hr[i] = xr; hi[i] = xi;
    }
    float Mr = ar, Mi = ai;
#pragma unroll
    for (int s = 0; s < 3; ++s) { float tr = Mr * Mr - Mi * Mi, ti = 2.f * Mr * Mi; Mr = tr; Mi = ti; }
    float vr = hr[7], vi = hi[7];
    float Lr = 1.f, Li = 0.f;
#pragma unroll
    for (int s = 0; s < 6; ++s) {
      int o = 1 << s;
      float qr = __shfl_up(vr, o), qi = __shfl_up(vi, o);
      if (lane >= o) {
        vr += Mr * qr - Mi * qi;
        vi += Mr * qi + Mi * qr;
      }
      if ((lane >> s) & 1) { float tr = Lr * Mr - Li * Mi, ti = Lr * Mi + Li * Mr; Lr = tr; Li = ti; }
      float tr = Mr * Mr - Mi * Mi, ti = 2.f * Mr * Mi; Mr = tr; Mi = ti;
    }
    if (lane == 63) wtot[wid] = make_float2(vr, vi);
    float er_ = __shfl_up(vr, 1), ei_ = __shfl_up(vi, 1);
    if (lane == 0) { er_ = 0.f; ei_ = 0.f; }
    __syncthreads();
    float c_r = 0.f, c_i = 0.f;
#pragma unroll
    for (int w = 0; w < 3; ++w) {
      if (w < wid) {
        float2 t = wtot[w];
        float tr = Mr * c_r - Mi * c_i + t.x, ti = Mr * c_i + Mi * c_r + t.y;
        c_r = tr; c_i = ti;
      }
    }
    float gr = er_ + Lr * c_r - Li * c_i, gi = ei_ + Lr * c_i + Li * c_r;
    bf16x8 ore, oim;
#pragma unroll
    for (int i = 0; i < 8; ++i) {
      float tr = ar * gr - ai * gi, ti = ar * gi + ai * gr;
      gr = tr; gi = ti;
      ore[i] = (short)f2bf(hr[i] + gr);
      oim[i] = (short)f2bf(-(hi[i] + gi));
    }
    *(bf16x8*)(hsT + (long)p * SEQ + tid * 8) = ore;
    *(bf16x8*)(hsT + (long)(PP + p) * SEQ + tid * 8) = oim;
  } else {
    long idx = ((long)(blockIdx.x - SCAN_BLOCKS) * 256 + threadIdx.x) * 4;
    f32x4 s = *(const f32x4*)(part + idx);
#pragma unroll
    for (int k = 1; k < KSPLIT; ++k) {
      f32x4 v = *(const f32x4*)(part + (long)k * DM * FF + idx);
      s += v;
    }
    int f = (int)(idx % FF);
    bf16x4 bb = *(const bf16x4*)(b1 + f);
    bf16x4 o;
#pragma unroll
    for (int j = 0; j < 4; ++j) {
      float v = rbf(rbf(s[j]) + bf2f((u16)bb[j]));
      o[j] = (short)f2bf(gelu_tanh(v));
    }
    *(bf16x4*)(u + idx) = o;
  }
  SPAN_END(2)
}

#define K3_NST 4
#define K3_PD 6
#define K3_STAGE 32768
#define K3_NT 1024
__global__ __launch_bounds__(K3_NT) void k_gemm3(const u16* __restrict__ hsT, const u16* __restrict__ Cre,
                                                  const u16* __restrict__ Cim, const u16* __restrict__ w2,
                                                  const u16* __restrict__ u, const u16* __restrict__ x,
                                                  const u16* __restrict__ xn, const u16* __restrict__ Dsk,
                                                  const u16* __restrict__ b2, float* __restrict__ out) {
  __shared__ __attribute__((aligned(16))) char smem[K3_NST * K3_STAGE];
  const int tid = threadIdx.x, lane = tid & 63, wid = tid >> 6;
  const int xcd = blockIdx.x & 7, loc = blockIdx.x >> 3;
  const int mt = xcd * 4 + (loc & 3), nt = loc >> 2;
  const int s0 = mt * 64, d0 = nt * 64;
  const int orow = tid >> 4, ocol = (tid & 15) * 4;
  const long o = (long)(s0 + orow) * DM + d0 + ocol;
  const bf16x4 xv = *(const bf16x4*)(x + o);
  const bf16x4 xnv = *(const bf16x4*)(xn + o);
  const bf16x4 dk = *(const bf16x4*)(Dsk + d0 + ocol);
  const float b2v = bf2f(b2[s0 + orow]);

  const int kg = wid & 3, mh = (wid >> 2) & 1, nh = wid >> 3;
  f32x16 acc1, acc2;
#pragma unroll
  for (int r = 0; r < 16; ++r) { acc1[r] = 0.f; acc2[r] = 0.f; }
  constexpr int nk1 = (2 * PP) / 128, nk2 = FF / 128, nk = nk1 + nk2;
  const int slot = tid & 511;
  const bool isA = tid < 512;
  const unsigned voA = (ks64_row(slot) * SEQ + ks64_col(slot)) * 2;
  const unsigned voB1 = (kc_row(slot) * PP + kc_col(slot)) * 2;
  const unsigned voB2 = (kc_row(slot) * FF + kc_col(slot)) * 2;
  auto stage = [&](int k, int buf) {
#pragma unroll
    for (int h2 = 0; h2 < 2; ++h2) {
      unsigned t = lds_addr(smem) + buf * K3_STAGE + h2 * 16384 + (isA ? 0 : 8192);
      const u16* src;
      unsigned vo;
      if (k < nk1) {
        int k64 = k * 2 + h2;
        const u16* cb = (k < nk1 / 2) ? Cre + k64 * 64 : Cim + (k64 - nk1) * 64;
        src = isA ? hsT + (long)(k64 * 64) * SEQ + s0 : cb + (long)d0 * PP;
        vo = isA ? voA : voB1;
      } else {
        int k64 = (k - nk1) * 2 + h2;
        src = isA ? w2 + (long)(k64 * 64) * SEQ + s0 : u + (long)d0 * FF + k64 * 64;
        vo = isA ? voA : voB2;
      }
      put(src, vo, t, slot);
    }
  };
  auto compute = [&](int k, int buf) {
    bf16x8 fa[2], fb[2];
#pragma unroll
    for (int hf = 0; hf < 2; ++hf) {
      const char* a = smem + buf * K3_STAGE + hf * 16384;
      const char* b = a + 8192;
      fa[hf] = frag_ks64(a, mh, kg, lane);
      fb[hf] = frag_kc(b, nh, kg, lane);
    }
    if (k < nk1) {
#pragma unroll
      for (int hf = 0; hf < 2; ++hf) acc1 = MFMA32(fa[hf], fb[hf], acc1);
    } else {
#pragma unroll
      for (int hf = 0; hf < 2; ++hf) acc2 = MFMA32(fa[hf], fb[hf], acc2);
    }
  };
  pipeline<K3_NST, 2>(nk, stage, compute);

  float* red = (float*)smem;
  __syncthreads();
#pragma unroll
  for (int r = 0; r < 16; ++r) {
    int row = mh * 32 + (r & 3) + 8 * (r >> 2) + 4 * (lane >> 5);
    int col = nh * 32 + (lane & 31);
    red[kg * 4096 + row * 64 + col] = acc1[r];
    red[(4 + kg) * 4096 + row * 64 + col] = acc2[r];
  }
  __syncthreads();
  f32x4 s1 = *(const f32x4*)(red + orow * 64 + ocol);
  f32x4 s2 = *(const f32x4*)(red + 4 * 4096 + orow * 64 + ocol);
#pragma unroll
  for (int w = 1; w < 4; ++w) {
    s1 += *(const f32x4*)(red + w * 4096 + orow * 64 + ocol);
    s2 += *(const f32x4*)(red + (4 + w) * 4096 + orow * 64 + ocol);
  }
  f32x4 res;
#pragma unroll
  for (int e = 0; e < 4; ++e) {
    float s = 2.0f * s1[e] + rbf(bf2f((u16)xnv[e]) * bf2f((u16)dk[e]));
    float y2 = rbf(rbf(s2[e]) + b2v);
    float g = rbf(y2 / (1.0f + expf(-y2)));
    res[e] = bf2f((u16)xv[e]) + s * g;
  }
  *(f32x4*)(out + o) = res;
}

#define K3B_NST 3
#define K3B_STAGE 49152
__global__ __launch_bounds__(1024) void k_gemm3b(const u16* __restrict__ hsT, const u16* __restrict__ Cre,
                                                 const u16* __restrict__ Cim, const u16* __restrict__ w2,
                                                 const u16* __restrict__ u, const u16* __restrict__ x,
                                                 const u16* __restrict__ xn, const u16* __restrict__ Dsk,
                                                 const u16* __restrict__ b2, float* __restrict__ out) {
  __shared__ __attribute__((aligned(16))) char smem[K3B_NST * K3B_STAGE];
  const int tid = threadIdx.x, lane = tid & 63, wid = tid >> 6;
  const int xcd = blockIdx.x & 7, loc = blockIdx.x >> 3;
  const int mt = xcd * 2 + (loc & 1), nt = loc >> 1;
  const int s0 = mt * 128, d0 = nt * 64;
  const int orow = tid >> 3, ocol = (tid & 7) * 8;
  const long o = (long)(s0 + orow) * DM + d0 + ocol;
  const bf16x8 xv = *(const bf16x8*)(x + o);
  const bf16x8 xnv = *(const bf16x8*)(xn + o);
  const bf16x8 dk = *(const bf16x8*)(Dsk + d0 + ocol);
  const float b2v = bf2f(b2[s0 + orow]);

  const int kg = wid & 1, mb = (wid >> 1) & 3, nb = wid >> 3;
  f32x16 acc1, acc2;
#pragma unroll
  for (int r = 0; r < 16; ++r) { acc1[r] = 0.f; acc2[r] = 0.f; }
  constexpr int nk1 = (2 * PP) / 128, nk2 = FF / 128, nk = nk1 + nk2;
  const int bslot = tid & 511, bh = tid >> 9;
  const unsigned voA = (ks128_row(tid) * SEQ + ks128_col(tid)) * 2;
  const unsigned voB1 = (kc_row(bslot) * PP + kc_col(bslot)) * 2;
  const unsigned voB2 = (kc_row(bslot) * FF + kc_col(bslot)) * 2;
  auto stage = [&](int k, int buf) {
    unsigned base = lds_addr(smem) + buf * K3B_STAGE;
    if (k < nk1) {
#pragma unroll
      for (int h2 = 0; h2 < 2; ++h2) put(hsT + (long)((k * 2 + h2) * 64) * SEQ + s0, voA, base + h2 * 16384, tid);
      int k64 = k * 2 + bh;
      const u16* cb = (k < nk1 / 2) ? Cre + k64 * 64 : Cim + (k64 - nk1) * 64;
      put(cb + (long)d0 * PP, voB1, base + 32768 + bh * 8192, bslot);
    } else {
      int kk = k - nk1;
#pragma unroll
      for (int h2 = 0; h2 < 2; ++h2) put(w2 + (long)((kk * 2 + h2) * 64) * SEQ + s0, voA, base + h2 * 16384, tid);
      put(u + (long)d0 * FF + (kk * 2 + bh) * 64, voB2, base + 32768 + bh * 8192, bslot);
    }
  };
  auto compute = [&](int k, int buf) {
    bf16x8 fa[4], fb[4];
#pragma unroll
    for (int j = 0; j < 4; ++j) {
      int q = kg + 2 * j, hf = q >> 2, ks = q & 3;
      fa[j] = frag_ks128(smem + buf * K3B_STAGE + hf * 16384, mb, ks, lane);
      fb[j] = frag_kc(smem + buf * K3B_STAGE + 32768 + hf * 8192, nb, ks, lane);
    }
    if (k < nk1) {
#pragma unroll
      for (int j = 0; j < 4; ++j) acc1 = MFMA32(fa[j], fb[j], acc1);
    } else {
#pragma unroll
      for (int j = 0; j < 4; ++j) acc2 = MFMA32(fa[j], fb[j], acc2);
    }
  };
  pipeline<K3B_NST, 3>(nk, stage, compute);
  float* red = (float*)smem;
  __syncthreads();
#pragma unroll
  for (int r = 0; r < 16; ++r) {
    int row = mb * 32 + (r & 3) + 8 * (r >> 2) + 4 * (lane >> 5);
    int col = nb * 32 + (lane & 31);
    red[kg * 8192 + row * 64 + col] = acc1[r];
    red[(2 + kg) * 8192 + row * 64 + col] = acc2[r];
  }
  __syncthreads();
#pragma unroll
  for (int c4 = 0; c4 < 2; ++c4) {
    f32x4 s1 = *(const f32x4*)(red + orow * 64 + ocol + c4 * 4) + *(const f32x4*)(red + 8192 + orow * 64 + ocol + c4 * 4);
    f32x4 s2 = *(const f32x4*)(red + 2 * 8192 + orow * 64 + ocol + c4 * 4) + *(const f32x4*)(red + 3 * 8192 + orow * 64 + ocol + c4 * 4);
    f32x4 res;
#pragma unroll
    for (int e = 0; e < 4; ++e) {
      float s = 2.0f * s1[e] + rbf(bf2f((u16)xnv[c4 * 4 + e]) * bf2f((u16)dk[c4 * 4 + e]));
      float y2 = rbf(rbf(s2[e]) + b2v);
      float g = rbf(y2 / (1.0f + expf(-y2)));
      res[e] = bf2f((u16)xv[c4 * 4 + e]) + s * g;
    }
    *(f32x4*)(out + o + c4 * 4) = res;
  }
}

extern "C" void kernel_launch(void* const* d_in, const int* in_sizes, int n_in, void* d_out, int out_size,
                              void* d_ws, size_t ws_size, hipStream_t stream) {
  const u16* x = (const u16*)d_in[0];
  const u16* Lre = (const u16*)d_in[1];
  const u16* Lim = (const u16*)d_in[2];
  const u16* Bre = (const u16*)d_in[3];
  const u16* Bim = (const u16*)d_in[4];
  const u16* Cre = (const u16*)d_in[5];
  const u16* Cim = (const u16*)d_in[6];
  const u16* logdt = (const u16*)d_in[7];
  const u16* Dsk = (const u16*)d_in[8];
  const u16* w1 = (const u16*)d_in[10];
  const u16* b1 = (const u16*)d_in[11];
  const u16* w2 = (const u16*)d_in[12];
  const u16* b2 = (const u16*)d_in[13];
  float* out = (float*)d_out;

  char* ws = (char*)d_ws;
  u16* xn = (u16*)ws;                       ws += (size_t)SEQ * DM * 2;
  u16* h = (u16*)ws;                        ws += (size_t)SEQ * DM * 2;
  float* Rt = (float*)ws;                   ws += (size_t)SEQ * 2 * PP * 4;
  float* part = (float*)ws;                 ws += (size_t)KSPLIT * DM * FF * 4;
  u16* u = (u16*)ws;                        ws += (size_t)DM * FF * 2;
  u16* hsT = (u16*)ws;                      ws += (size_t)SEQ * 2 * PP * 2;

  k_ln<<<SEQ / 4 + (FF * SEQ * 2) / (256 * 8 * 16), 256, 0, stream>>>(x, xn, h, w1);
  k_gemm1<<<K1_BLOCKS, K1_NT, 0, stream>>>(xn, h, Bre, Bim, w1, Rt, part);
  k_scan<<<SCAN_BLOCKS + RED_BLOCKS + (FF * SEQ * 2) / (256 * 8 * 16), 256, 0, stream>>>(Rt, Lre, Lim, logdt, hsT, part, b1, u, w2);
  k_gemm3<<<256, K3_NT, 0, stream>>>(hsT, Cre, Cim, w2, u, x, xn, Dsk, b2, out);
}
```

```cpp
#include <hip/hip_runtime.h>
#include <hip/hip_bf16.h>

#define SEQ 2048
#define DM 512
#define PP 512
#define FF 1792

typedef unsigned short u16;
typedef __attribute__((ext_vector_type(8))) short bf16x8;
typedef __attribute__((ext_vector_type(4))) short bf16x4;
typedef __attribute__((ext_vector_type(16))) float f32x16;
typedef __attribute__((ext_vector_type(4))) float f32x4;
typedef __attribute__((address_space(3))) const char* lds_cptr;
typedef __attribute__((address_space(3))) bf16x4 lds_bf16x4;

__device__ __forceinline__ float bf2f(u16 v) { return __uint_as_float(((unsigned)v) << 16); }
__device__ __forceinline__ u16 f2bf(float f) {
  unsigned u = __float_as_uint(f);
  u += 0x7fffu + ((u >> 16) & 1u);
  return (u16)(u >> 16);
}
__device__ __forceinline__ float rbf(float f) { return bf2f(f2bf(f)); }

__device__ __forceinline__ float wave_sum(float v) {
#pragma unroll
  for (int o = 32; o > 0; o >>= 1) v += __shfl_xor(v, o);
  return v;
}

__device__ __forceinline__ void ln_bf16(const float (&in)[8], float (&out)[8]) {
  float s = 0.f;
#pragma unroll
  for (int j = 0; j < 8; ++j) s += in[j];
  s = wave_sum(s);
  float mean = s * (1.0f / DM);
  float q = 0.f;
#pragma unroll
  for (int j = 0; j < 8; ++j) { float d = in[j] - mean; q += d * d; }
  q = wave_sum(q);
  float var = rbf(q * (1.0f / DM));
  float m = rbf(mean);
  float sd = rbf(sqrtf(rbf(var + 1e-5f)));
#pragma unroll
  for (int j = 0; j < 8; ++j) out[j] = rbf(rbf(in[j] - m) / sd);
}

__global__ __launch_bounds__(256) void k_ln(const u16* __restrict__ x, u16* __restrict__ xn, u16* __restrict__ h,
                                            const u16* __restrict__ w1pf) {
  int row = blockIdx.x * 4 + (threadIdx.x >> 6);
  int lane = threadIdx.x & 63;
  const u16* xr = x + (long)row * DM + lane * 8;
  bf16x8 v = *(const bf16x8*)xr;
  float in[8], o1[8], o2[8];
#pragma unroll
  for (int j = 0; j < 8; ++j) in[j] = bf2f((u16)v[j]);
  ln_bf16(in, o1);
  ln_bf16(o1, o2);
  bf16x8 w1, w2;
#pragma unroll
  for (int j = 0; j < 8; ++j) { w1[j] = (short)f2bf(o1[j]); w2[j] = (short)f2bf(o2[j]); }
  *(bf16x8*)(xn + (long)row * DM + lane * 8) = w1;
  *(bf16x8*)(h + (long)row * DM + lane * 8) = w2;
}

__device__ __forceinline__ void glds16(const void* sbase, unsigned voff, unsigned lds_wave_base) {
  unsigned keep;
  asm volatile("s_mov_b32 %0, m0\n\ts_mov_b32 m0, %3\n\ts_nop 0\n\tglobal_load_lds_dwordx4 %1, %2\n\ts_mov_b32 m0, %0"
               : "=&s"(keep) : "v"(voff), "s"(sbase), "s"(lds_wave_base) : "memory");
}
__device__ __forceinline__ unsigned lds_addr(const void* p) { return (unsigned)(size_t)p; }
__device__ __forceinline__ unsigned uni(unsigned v) { return __builtin_amdgcn_readfirstlane(v); }
__device__ __forceinline__ const void* uni_ptr(const void* p) {
  unsigned long long v = (unsigned long long)p;
  unsigned lo = __builtin_amdgcn_readfirstlane((unsigned)v), hi = __builtin_amdgcn_readfirstlane((unsigned)(v >> 32));
  return (const void*)(((unsigned long long)hi << 32) | lo);
}

__device__ __forceinline__ int kc_row(int slot) { return slot >> 3; }
__device__ __forceinline__ int kc_col(int slot) { int row = slot >> 3; return ((slot & 7) ^ ((row >> 1) & 7)) * 8; }
__device__ __forceinline__ int ks128_row(int slot) { return slot >> 4; }
__device__ __forceinline__ int ks128_col(int slot) { int row = slot >> 4; return ((slot & 15) ^ (((row & 3) << 2) | ((row >> 2) & 3))) * 8; }
__device__ __forceinline__ int ks64_row(int slot) { return (slot >> 6) * 8 + ((slot >> 2) & 7); }
__device__ __forceinline__ int ks64_col(int slot) { int row = ks64_row(slot); return (4 * ((slot >> 5) & 1) + ((slot & 3) ^ ((row >> 2) & 3))) * 8; }
__device__ __forceinline__ void put(const void* sbase, unsigned voff, unsigned tile, int slot) { glds16(uni_ptr(sbase), voff, uni(tile + (slot & ~63) * 16)); }

__device__ __forceinline__ bf16x8 frag_kc(const char* lds, int blk, int ks, int lane) {
  int row = blk * 32 + (lane & 31);
  int ch = ks * 2 + (lane >> 5);
  return *(const bf16x8*)(lds + row * 128 + ((ch ^ ((row >> 1) & 7)) << 4));
}
__device__ __forceinline__ bf16x8 frag_ks128(const char* lds, int blk, int ks, int lane) {
  int hh = lane >> 5, b2 = (lane >> 4) & 1, q = (lane & 15) >> 2, p = lane & 3;
  bf16x8 r;
#pragma unroll
  for (int t = 0; t < 2; ++t) {
    int row = 16 * ks + 8 * hh + 4 * t + q;
    int ch = 4 * blk + 2 * b2 + (p >> 1);
    int off = 256 * row + 16 * (ch ^ (((row & 3) << 2) | ((row >> 2) & 3))) + 8 * (p & 1);
    bf16x4 v = __builtin_amdgcn_ds_read_tr16_b64_v4i16((lds_bf16x4*)(lds_cptr)(lds + off));
    r[4 * t + 0] = v[0]; r[4 * t + 1] = v[1]; r[4 * t + 2] = v[2]; r[4 * t + 3] = v[3];
  }
  return r;
}
__device__ __forceinline__ bf16x8 frag_ks64(const char* lds, int blk, int ks, int lane) {
  int hh = lane >> 5, b2 = (lane >> 4) & 1, q = (lane & 15) >> 2, p = lane & 3;
  bf16x8 r;
#pragma unroll
  for (int t = 0; t < 2; ++t) {
    int row = 16 * ks + 8 * hh + 4 * t + q;
    int ch = 4 * blk + 2 * b2 + (p >> 1);
    int off = 1024 * (row >> 3) + 512 * (ch >> 2) + 64 * (row & 7) + 16 * ((ch & 3) ^ ((row >> 2) & 3)) + 8 * (p & 1);
    bf16x4 v = __builtin_amdgcn_ds_read_tr16_b64_v4i16((lds_bf16x4*)(lds_cptr)(lds + off));
    r[4 * t + 0] = v[0]; r[4 * t + 1] = v[1]; r[4 * t + 2] = v[2]; r[4 * t + 3] = v[3];
  }
  return r;
}

#define ST_WT(p, v) __hip_atomic_store((p), (v), __ATOMIC_RELAXED, __HIP_MEMORY_SCOPE_AGENT)
#define MFMA32(a, b, c) __builtin_amdgcn_mfma_f32_32x32x16_bf16(a, b, c, 0, 0, 0)

template <int NST, int LPS, class StageF, class ComputeF>
__device__ __forceinline__ void pipeline(int nk, StageF stage, ComputeF compute) {
#pragma unroll
  for (int s = 0; s < NST - 1; ++s)
    if (s < nk) stage(s, s);
  for (int k = 0; k < nk; ++k) {
    if (k + NST - 2 < nk) {
      asm volatile("s_waitcnt vmcnt(%0)" ::"n"((NST - 2) * LPS) : "memory");
    } else {
      asm volatile("s_waitcnt vmcnt(0)" ::: "memory");
    }
    __builtin_amdgcn_s_barrier();
    if (k + NST - 1 < nk) stage(k + NST - 1, (k + NST - 1) % NST);
    compute(k, k % NST);
  }
}

#define KSPLIT 4
#define K1_BLOCKS (128 + 4 * 14 * KSPLIT)
#define K1_NST 2
#define K1_NT 512

__global__ __launch_bounds__(K1_NT, 4) void k_gemm1(const u16* __restrict__ xn, const u16* __restrict__ h,
                                                   const u16* __restrict__ Bre, const u16* __restrict__ Bim,
                                                   const u16* __restrict__ w1, float* __restrict__ Rt,
                                                   float* __restrict__ part) {
  __shared__ __attribute__((aligned(16))) char smem[K1_NST * 32768];
  const int tid = threadIdx.x, lane = tid & 63, wid = tid >> 6, wm = wid >> 2, wn = wid & 3;
  f32x16 acc[2];
#pragma unroll
  for (int i = 0; i < 2; ++i)
#pragma unroll
    for (int r = 0; r < 16; ++r) acc[i][r] = 0.f;

  const int xcd = blockIdx.x & 7, loc = blockIdx.x >> 3;
  if (loc >= 28) {
    const int l = loc - 28;
    const int mt = l & 7, nt = xcd * 2 + (l >> 3);
    const int m0 = mt * 128, t0 = nt * 128;
    const u16* bbase = (mt < 4) ? Bre + (long)m0 * DM : Bim + (long)(m0 - PP) * DM;
    auto stage = [&](int k, int buf) {
      unsigned a = lds_addr(smem) + buf * 32768;
      unsigned b = a + 16384;
#pragma unroll
      for (int i = 0; i < 2; ++i) {
        int slot = i * K1_NT + tid;
        unsigned vo = (kc_row(slot) * DM + kc_col(slot)) * 2;
        put(bbase + k * 64, vo, a, slot);
        put(xn + (long)t0 * DM + k * 64, vo, b, slot);
      }
    };
    auto compute = [&](int k, int buf) {
      const char* a = smem + buf * 32768;
      const char* b = a + 16384;
#pragma unroll
      for (int ks = 0; ks < 4; ++ks) {
        bf16x8 fa[2], fb;
#pragma unroll
        for (int i = 0; i < 2; ++i) fa[i] = frag_kc(a, wm * 2 + i, ks, lane);
        fb = frag_kc(b, wn, ks, lane);
#pragma unroll
        for (int i = 0; i < 2; ++i) acc[i] = MFMA32(fa[i], fb, acc[i]);
      }
    };
    pipeline<K1_NST, 4>(DM / 64, stage, compute);
#pragma unroll
    for (int i = 0; i < 2; ++i)
#pragma unroll
      for (int r = 0; r < 16; ++r) {
        int row = m0 + wm * 64 + i * 32 + (r & 3) + 8 * (r >> 2) + 4 * (lane >> 5);
        int col = t0 + wn * 32 + (lane & 31);
        ST_WT(&Rt[(long)row * SEQ + col], acc[i][r]);
      }
  } else {
    const int ksp = xcd >> 1;
    const int mt = (xcd & 1) * 2 + (loc & 1), nt = loc >> 1;
    const int d0 = mt * 128, f0 = nt * 128;
    const int kbeg = ksp * (SEQ / KSPLIT);
    auto stage = [&](int k, int buf) {
      unsigned a = lds_addr(smem) + buf * 32768;
      unsigned bb = a + 16384;
#pragma unroll
      for (int i = 0; i < 2; ++i) {
        int slot = i * K1_NT + tid;
        put(h + (long)(kbeg + k * 64) * DM + d0, (ks128_row(slot) * DM + ks128_col(slot)) * 2, a, slot);
        put(w1 + (long)(kbeg + k * 64) * FF + f0, (ks128_row(slot) * FF + ks128_col(slot)) * 2, bb, slot);
      }
    };
    auto compute = [&](int k, int buf) {
      const char* a = smem + buf * 32768;
      const char* bb = a + 16384;
#pragma unroll
      for (int ks = 0; ks < 4; ++ks) {
        bf16x8 fa[2], fb;
#pragma unroll
        for (int i = 0; i < 2; ++i) fa[i] = frag_ks128(a, wm * 2 + i, ks, lane);
        fb = frag_ks128(bb, wn, ks, lane);
#pragma unroll
        for (int i = 0; i < 2; ++i) acc[i] = MFMA32(fa[i], fb, acc[i]);
      }
    };
    pipeline<K1_NST, 4>(SEQ / KSPLIT / 64, stage, compute);
    float* po = part + (long)ksp * DM * FF;
#pragma unroll
    for (int i = 0; i < 2; ++i)
#pragma unroll
      for (int r = 0; r < 16; ++r) {
        int row = d0 + wm * 64 + i * 32 + (r & 3) + 8 * (r >> 2) + 4 * (lane >> 5);
        int col = f0 + wn * 32 + (lane & 31);
        ST_WT(&po[(long)row * FF + col], acc[i][r]);
      }
  }
}

#define SCAN_BLOCKS PP
#define RED_BLOCKS (DM * FF / (256 * 4))

__device__ __forceinline__ float gelu_tanh(float x) {
  const float k0 = 0.7978845608028654f, k1 = 0.044715f;
  float inner = k0 * (x + k1 * x * x * x);
  return 0.5f * x * (1.f + tanhf(inner));
}

__global__ __launch_bounds__(256) void k_scan(const float* __restrict__ Rt, const u16* __restrict__ Lre,
                                               const u16* __restrict__ Lim, const u16* __restrict__ logdt,
                                               u16* __restrict__ hsT, const float* __restrict__ part,
                                               const u16* __restrict__ b1, u16* __restrict__ u,
                                               const u16* __restrict__ w2pf) {
  __shared__ float2 wtot[4];
  if (blockIdx.x >= SCAN_BLOCKS + RED_BLOCKS) {
    const int b = blockIdx.x - (SCAN_BLOCKS + RED_BLOCKS);
    const uint4* p = (const uint4*)w2pf + (long)b * 256 * 8 + threadIdx.x;
    uint4 acc4 = p[0];
#pragma unroll
    for (int i = 1; i < 8; ++i) { uint4 v = p[i * 256]; acc4.x ^= v.x; acc4.y ^= v.y; acc4.z ^= v.z; acc4.w ^= v.w; }
    if ((acc4.x ^ acc4.y ^ acc4.z ^ acc4.w) == 0x12345678u) wtot[0].x = 1.f;
    return;
  }
  if (blockIdx.x < SCAN_BLOCKS) {
    const int p = blockIdx.x;
    const int tid = threadIdx.x, lane = tid & 63, wid = tid >> 6;
    const float* pr = Rt + (long)p * SEQ + tid * 8;
    const float* pi = Rt + (long)(PP + p) * SEQ + tid * 8;
    f32x4 vr0 = *(const f32x4*)pr, vr1 = *(const f32x4*)(pr + 4);
    f32x4 vi0 = *(const f32x4*)pi, vi1 = *(const f32x4*)(pi + 4);
    float dt = rbf(expf(bf2f(logdt[p])));
    float lr = bf2f(Lre[p]), li = bf2f(Lim[p]);
    float er = expf(dt * lr);
    float sn, cs;
    sincosf(dt * li, &sn, &cs);
    float ar = er * cs, ai = er * sn;
    float den = lr * lr + li * li;
    float nr = ar - 1.0f, ni = ai;
    float cr = (nr * lr + ni * li) / den, ci = (ni * lr - nr * li) / den;
    float hr[8], hi[8];
#pragma unroll
    for (int i = 0; i < 8; ++i) {
      float x = i < 4 ? vr0[i & 3] : vr1[i & 3];
      float y = i < 4 ? vi0[i & 3] : vi1[i & 3];
      hr[i] = cr * x - ci * y;
      hi[i] = cr * y + ci * x;
    }
#pragma unroll
    for (int i = 1; i < 8; ++i) {
      float xr = ar * hr[i - 1] - ai * hi[i - 1] + hr[i];
      float xi = ar * hi[i - 1] + ai * hr[i - 1] + hi[i];
      hr[i] = xr; hi[i] = xi;
    }
    float Mr = ar, Mi = ai;
#pragma unroll
    for (int s = 0; s < 3; ++s) { float tr = Mr * Mr - Mi * Mi, ti = 2.f * Mr * Mi; Mr = tr; Mi = ti; }
    float vr = hr[7], vi = hi[7];
    float Lr = 1.f, Li = 0.f;
#pragma unroll
    for (int s = 0; s < 6; ++s) {
      int o = 1 << s;
      float qr = __shfl_up(vr, o), qi = __shfl_up(vi, o);
      if (lane >= o) {
        vr += Mr * qr - Mi * qi;
        vi += Mr * qi + Mi * qr;
      }
      if ((lane >> s) & 1) { float tr = Lr * Mr - Li * Mi, ti = Lr * Mi + Li * Mr; Lr = tr; Li = ti; }
      float tr = Mr * Mr - Mi * Mi, ti = 2.f * Mr * Mi; Mr = tr; Mi = ti;
    }
    if (lane == 63) wtot[wid] = make_float2(vr, vi);
    float er_ = __shfl_up(vr, 1), ei_ = __shfl_up(vi, 1);
    if (lane == 0) { er_ = 0.f; ei_ = 0.f; }
    __syncthreads();
    float c_r = 0.f, c_i = 0.f;
#pragma unroll
    for (int w = 0; w < 3; ++w) {
      if (w < wid) {
        float2 t = wtot[w];
        float tr = Mr * c_r - Mi * c_i + t.x, ti = Mr * c_i + Mi * c_r + t.y;
        c_r = tr; c_i = ti;
      }
    }
    float gr = er_ + Lr * c_r - Li * c_i, gi = ei_ + Lr * c_i + Li * c_r;
    bf16x8 ore, oim;
#pragma unroll
    for (int i = 0; i < 8; ++i) {
      float tr = ar * gr - ai * gi, ti = ar * gi + ai * gr;
      gr = tr; gi = ti;
      ore[i] = (short)f2bf(hr[i] + gr);
      oim[i] = (short)f2bf(-(hi[i] + gi));
    }
    *(bf16x8*)(hsT + (long)p * SEQ + tid * 8) = ore;
    *(bf16x8*)(hsT + (long)(PP + p) * SEQ + tid * 8) = oim;
  } else {
    long idx = ((long)(blockIdx.x - SCAN_BLOCKS) * 256 + threadIdx.x) * 4;
    f32x4 s = *(const f32x4*)(part + idx);
#pragma unroll
    for (int k = 1; k < KSPLIT; ++k) {
      f32x4 v = *(const f32x4*)(part + (long)k * DM * FF + idx);
      s += v;
    }
    int f = (int)(idx % FF);
    bf16x4 bb = *(const bf16x4*)(b1 + f);
    bf16x4 o;
#pragma unroll
    for (int j = 0; j < 4; ++j) {
      float v = rbf(rbf(s[j]) + bf2f((u16)bb[j]));
      o[j] = (short)f2bf(gelu_tanh(v));
    }
    *(bf16x4*)(u + idx) = o;
  }
}

#define K3_NST 4
#define K3_PD 6
#define K3_STAGE 32768
#define K3_NT 1024
__global__ __launch_bounds__(K3_NT) void k_gemm3(const u16* __restrict__ hsT, const u16* __restrict__ Cre,
                                                  const u16* __restrict__ Cim, const u16* __restrict__ w2,
                                                  const u16* __restrict__ u, const u16* __restrict__ x,
                                                  const u16* __restrict__ xn, const u16* __restrict__ Dsk,
                                                  const u16* __restrict__ b2, float* __restrict__ out) {
  __shared__ __attribute__((aligned(16))) char smem[K3_NST * K3_STAGE];
  const int tid = threadIdx.x, lane = tid & 63, wid = tid >> 6;
  const int xcd = blockIdx.x & 7, loc = blockIdx.x >> 3;
  const int mt = xcd * 4 + (loc & 3), nt = loc >> 2;
  const int s0 = mt * 64, d0 = nt * 64;
  const int orow = tid >> 4, ocol = (tid & 15) * 4;
  const long o = (long)(s0 + orow) * DM + d0 + ocol;
  const bf16x4 xv = *(const bf16x4*)(x + o);
  const bf16x4 xnv = *(const bf16x4*)(xn + o);
  const bf16x4 dk = *(const bf16x4*)(Dsk + d0 + ocol);
  const float b2v = bf2f(b2[s0 + orow]);

  const int kg = wid & 3, mh = (wid >> 2) & 1, nh = wid >> 3;
  f32x16 acc1, acc2;
#pragma unroll
  for (int r = 0; r < 16; ++r) { acc1[r] = 0.f; acc2[r] = 0.f; }
  constexpr int nk1 = (2 * PP) / 128, nk2 = FF / 128, nk = nk1 + nk2;
  const int slot = tid & 511;
  const bool isA = tid < 512;
  const unsigned voA = (ks64_row(slot) * SEQ + ks64_col(slot)) * 2;
  const unsigned voB1 = (kc_row(slot) * PP + kc_col(slot)) * 2;
  const unsigned voB2 = (kc_row(slot) * FF + kc_col(slot)) * 2;
  auto stage = [&](int k, int buf) {
#pragma unroll
    for (int h2 = 0; h2 < 2; ++h2) {
      unsigned t = lds_addr(smem) + buf * K3_STAGE + h2 * 16384 + (isA ? 0 : 8192);
      const u16* src;
      unsigned vo;
      if (k < nk1) {
        int k64 = k * 2 + h2;
        const u16* cb = (k < nk1 / 2) ? Cre + k64 * 64 : Cim + (k64 - nk1) * 64;
        src = isA ? hsT + (long)(k64 * 64) * SEQ + s0 : cb + (long)d0 * PP;
        vo = isA ? voA : voB1;
      } else {
        int k64 = (k - nk1) * 2 + h2;
        src = isA ? w2 + (long)(k64 * 64) * SEQ + s0 : u + (long)d0 * FF + k64 * 64;
        vo = isA ? voA : voB2;
      }
      put(src, vo, t, slot);
    }
  };
  auto compute = [&](int k, int buf) {
    bf16x8 fa[2], fb[2];
#pragma unroll
    for (int hf = 0; hf < 2; ++hf) {
      const char* a = smem + buf * K3_STAGE + hf * 16384;
      const char* b = a + 8192;
      fa[hf] = frag_ks64(a, mh, kg, lane);
      fb[hf] = frag_kc(b, nh, kg, lane);
    }
    if (k < nk1) {
#pragma unroll
      for (int hf = 0; hf < 2; ++hf) acc1 = MFMA32(fa[hf], fb[hf], acc1);
    } else {
#pragma unroll
      for (int hf = 0; hf < 2; ++hf) acc2 = MFMA32(fa[hf], fb[hf], acc2);
    }
  };
  pipeline<K3_NST, 2>(nk, stage, compute);

  float* red = (float*)smem;
  __syncthreads();
#pragma unroll
  for (int r = 0; r < 16; ++r) {
    int row = mh * 32 + (r & 3) + 8 * (r >> 2) + 4 * (lane >> 5);
    int col = nh * 32 + (lane & 31);
    red[kg * 4096 + row * 64 + col] = acc1[r];
    red[(4 + kg) * 4096 + row * 64 + col] = acc2[r];
  }
  __syncthreads();
  f32x4 s1 = *(const f32x4*)(red + orow * 64 + ocol);
  f32x4 s2 = *(const f32x4*)(red + 4 * 4096 + orow * 64 + ocol);
#pragma unroll
  for (int w = 1; w < 4; ++w) {
    s1 += *(const f32x4*)(red + w * 4096 + orow * 64 + ocol);
    s2 += *(const f32x4*)(red + (4 + w) * 4096 + orow * 64 + ocol);
  }
  f32x4 res;
#pragma unroll
  for (int e = 0; e < 4; ++e) {
    float s = 2.0f * s1[e] + rbf(bf2f((u16)xnv[e]) * bf2f((u16)dk[e]));
    float y2 = rbf(rbf(s2[e]) + b2v);
    float g = rbf(y2 / (1.0f + expf(-y2)));
    res[e] = bf2f((u16)xv[e]) + s * g;
  }
  *(f32x4*)(out + o) = res;
}

extern "C" void kernel_launch(void* const* d_in, const int* in_sizes, int n_in, void* d_out, int out_size,
                              void* d_ws, size_t ws_size, hipStream_t stream) {
  const u16* x = (const u16*)d_in[0];
  const u16* Lre = (const u16*)d_in[1];
  const u16* Lim = (const u16*)d_in[2];
  const u16* Bre = (const u16*)d_in[3];
  const u16* Bim = (const u16*)d_in[4];
  const u16* Cre = (const u16*)d_in[5];
  const u16* Cim = (const u16*)d_in[6];
  const u16* logdt = (const u16*)d_in[7];
  const u16* Dsk = (const u16*)d_in[8];
  const u16* w1 = (const u16*)d_in[10];
  const u16* b1 = (const u16*)d_in[11];
  const u16* w2 = (const u16*)d_in[12];
  const u16* b2 = (const u16*)d_in[13];
  float* out = (float*)d_out;

  char* ws = (char*)d_ws;
  u16* xn = (u16*)ws;                       ws += (size_t)SEQ * DM * 2;
  u16* h = (u16*)ws;                        ws += (size_t)SEQ * DM * 2;
  float* Rt = (float*)ws;                   ws += (size_t)SEQ * 2 * PP * 4;
  float* part = (float*)ws;                 ws += (size_t)KSPLIT * DM * FF * 4;
  u16* u = (u16*)ws;                        ws += (size_t)DM * FF * 2;
  u16* hsT = (u16*)ws;                      ws += (size_t)SEQ * 2 * PP * 2;

  k_ln<<<SEQ / 4, 256, 0, stream>>>(x, xn, h, w1);
  k_gemm1<<<K1_BLOCKS, K1_NT, 0, stream>>>(xn, h, Bre, Bim, w1, Rt, part);
  k_scan<<<SCAN_BLOCKS + RED_BLOCKS + (FF * SEQ * 2) / (256 * 8 * 16), 256, 0, stream>>>(Rt, Lre, Lim, logdt, hsT, part, b1, u, w2);
  k_gemm3<<<256, K3_NT, 0, stream>>>(hsT, Cre, Cim, w2, u, x, xn, Dsk, b2, out);
}
```
